```python
import torch

def _softmax(x, axis=-1):
    return torch.softmax(x, dim=axis)
def _gelu(x):
    return torch.nn.functional.gelu(x, approximate="tanh")
def _silu(x):
    return torch.nn.functional.silu(x)
def _sigmoid(x):
    return torch.sigmoid(x)
def _layernorm(x, axis=-1, eps=1e-5):
    m = x.mean(dim=axis, keepdim=True)
    v = x.var(dim=axis, keepdim=True, unbiased=False)
    return (x - m) / torch.sqrt(v + eps)
def _scatter_add(tgt, idx, src, axis=0):
    return tgt.clone().index_add_(axis, idx, src)
def _segment_sum(src, seg, n):
    out = torch.zeros((n,) + src.shape[1:], dtype=src.dtype)
    return out.index_add_(0, seg, src)
def _pad(x, pw):
    flat = tuple(v for pair in reversed(pw) for v in pair)
    return torch.nn.functional.pad(x, flat)

kernel_name = 'standalone__dense_activation_relu__prompt'

def setup_inputs():

    _w = (lambda a: (lambda t: t.to(torch.bfloat16) if t.is_floating_point() else t)(torch.as_tensor(a)))
    B, S, D, Dff = 1, 2048, 640, 1536
    return {
        "x": _w((lambda *s: torch.randn(*s, dtype=torch.bfloat16) * 0.05)(B, S, D)),
        "w1": _w((lambda *s: torch.randn(*s, dtype=torch.bfloat16) * 0.05)(D, Dff)),
        "w2": _w((lambda *s: torch.randn(*s, dtype=torch.bfloat16) * 0.05)(Dff, D)),
    }

def reference(x, w1, w2):
    h = torch.einsum("bsd,df->bsf", x, w1)
    zero = torch.zeros_like(h)
    h = torch.maximum(h, zero)
    return torch.einsum("bsf,fd->bsd", h, w2)
```

```cpp
#include <hip/hip_runtime.h>
#include <hip/hip_bf16.h>

typedef unsigned v4u __attribute__((ext_vector_type(4)));
typedef unsigned v2u __attribute__((ext_vector_type(2)));
typedef short s16x4 __attribute__((ext_vector_type(4)));
typedef __bf16 bf16x8 __attribute__((ext_vector_type(8)));
typedef __bf16 bf16x2 __attribute__((ext_vector_type(2)));
typedef float f32x2 __attribute__((ext_vector_type(2)));
typedef float f32x4 __attribute__((ext_vector_type(4)));
typedef float f32x16 __attribute__((ext_vector_type(16)));
#define LAS __attribute__((address_space(3)))
#define GAS __attribute__((address_space(1)))

constexpr int M_ = 2048, D_ = 640, F_ = 1536;
constexpr unsigned H_OFF = 65536;
constexpr unsigned H_BYTES = 128u * 48u * 1024u;
constexpr unsigned W2T_OFF = H_OFF + H_BYTES;
constexpr unsigned W2T_BYTES = 40u * 48u * 1024u;
constexpr unsigned WS_BYTES = W2T_OFF + W2T_BYTES;

constexpr unsigned XBASE = 0;
constexpr unsigned RING = 81920;
constexpr unsigned SLOT = 12288;
constexpr unsigned LDS_BYTES = 163840;

__device__ __forceinline__ void glds(__amdgpu_buffer_rsrc_t rs, unsigned voff, unsigned soff, unsigned ldsaddr) {
  asm volatile("s_mov_b32 m0, %3\n\ts_nop 0\n\tbuffer_load_dwordx4 %0, %1, %2 offen lds" ::"v"(voff), "s"(rs), "s"(soff), "s"(ldsaddr) : "memory");
}
__device__ __forceinline__ unsigned pk_relu(float a, float b) {
  f32x2 q = {a, b};
  bf16x2 r = __builtin_convertvector(q, bf16x2);
  typedef short s16x2 __attribute__((ext_vector_type(2)));
  s16x2 s = __builtin_bit_cast(s16x2, r);
  s16x2 z = {0, 0};
  s = __builtin_elementwise_max(s, z);
  return __builtin_bit_cast(unsigned, s);
}
__device__ __forceinline__ unsigned pk(float a, float b) {
  f32x2 q = {a, b};
  bf16x2 r = __builtin_convertvector(q, bf16x2);
  return __builtin_bit_cast(unsigned, r);
}
#define WAITV(n) asm volatile("s_waitcnt vmcnt(" #n ")" ::: "memory")
#define WAITL(n) asm volatile("s_waitcnt lgkmcnt(" #n ")" ::: "memory")

__global__ __launch_bounds__(256, 1) void k1(const unsigned char* __restrict__ x, const unsigned char* __restrict__ w1,
                                             const unsigned char* __restrict__ w2, unsigned char* __restrict__ ws) {
  extern __shared__ __attribute__((aligned(16))) unsigned char smem[];
  const int tid = threadIdx.x, l = tid & 63, w = __builtin_amdgcn_readfirstlane(tid >> 6);
  const int b = blockIdx.x, xcd = b & 7, r = b >> 3;
  const int t = (xcd >> 1) * 8 + (r >> 2), ft = (xcd & 1) * 4 + (r & 3);
  const __amdgpu_buffer_rsrc_t rx = __builtin_amdgcn_make_buffer_rsrc((void*)x, 0, M_ * D_ * 2, 0x00020000);
  const __amdgpu_buffer_rsrc_t rw1 = __builtin_amdgcn_make_buffer_rsrc((void*)w1, 0, D_ * F_ * 2, 0x00020000);
  const __amdgpu_buffer_rsrc_t rw2 = __builtin_amdgcn_make_buffer_rsrc((void*)w2, 0, F_ * D_ * 2, 0x00020000);
  const __amdgpu_buffer_rsrc_t rws = __builtin_amdgcn_make_buffer_rsrc((void*)ws, 0, WS_BYTES, 0x00020000);
  const unsigned lbase = (unsigned)(size_t)(LAS unsigned char*)smem;

  {
    int u = -1;
    if (w == 0) u = b; else if (w == 1 && b < 128) u = 256 + b;
    if (u >= 0) {
      const int kb = u >> 3, j = u & 7;
      const unsigned st = lbase + RING + 5 * SLOT + w * 5120;
      #pragma unroll
      for (int ii = 0; ii < 5; ++ii) {
        int c = ii * 64 + l, row = c / 10, ch = c % 10;
        glds(rw2, (unsigned)((kb * 32 + row) * 1280 + j * 160 + ch * 16), 0, st + ii * 1024);
      }
      WAITV(0);
      const int g = l >> 4, q = (l & 15) >> 2, p = l & 3;
      #pragma unroll
      for (int nt = 0; nt < 5; ++nt) {
        unsigned a0 = st + (16 * (g >> 1) + 4 * (g & 1) + q) * 160 + nt * 32 + p * 8;
        s16x4 lo = __builtin_amdgcn_ds_read_tr16_b64_v4i16((LAS s16x4*)(size_t)a0);
        s16x4 hi = __builtin_amdgcn_ds_read_tr16_b64_v4i16((LAS s16x4*)(size_t)(a0 + 8 * 160));
        v4u v = __builtin_bit_cast(v4u, __builtin_shufflevector(lo, hi, 0, 1, 2, 3, 4, 5, 6, 7));
        __builtin_amdgcn_raw_buffer_store_b128(v, rws, W2T_OFF + ((5 * j + nt) * 48 + kb) * 1024 + l * 16, 0, 0);
      }
    }
    __syncthreads();
  }

  unsigned vx[2];
  #pragma unroll
  for (int e = 0; e < 2; ++e) vx[e] = (unsigned)((t * 64 + 8 * (2 * w + e) + (l >> 3)) * 1280 + (((l & 7) ^ (4 * e + (l >> 4))) * 16));
  const unsigned vw = (unsigned)((8 * w + (l >> 3)) * 3072 + ft * 384 + (((l & 7) ^ (4 * ((l >> 4) & 1))) * 16));
  auto issue_x = [&](int c) {
    glds(rx, vx[0], c * 128, lbase + XBASE + c * 8192 + (2 * w) * 1024);
    glds(rx, vx[1], c * 128, lbase + XBASE + c * 8192 + (2 * w + 1) * 1024);
  };
  auto issue_w = [&](int s) {
    #pragma unroll
    for (int c3 = 0; c3 < 3; ++c3) glds(rw1, vw, s * 32 * 3072 + c3 * 128, lbase + RING + (s % 6) * SLOT + c3 * 4096 + w * 1024);
  };
  #pragma unroll
  for (int c = 0; c < 10; ++c) issue_x(c);
  #pragma unroll
  for (int s = 0; s < 5; ++s) issue_w(s);

  const int wm = w >> 1, wf = w & 1;
  const int h = l >> 5, blk = (l >> 4) & 1, q = (l & 15) >> 2, p = l & 3;
  unsigned aw[3];
  #pragma unroll
  for (int i = 0; i < 3; ++i) {
    int f32t = 3 * wf + i, c3 = f32t >> 1, half = f32t & 1;
    aw[i] = lbase + RING + c3 * 4096 + (8 * h + q) * 128 + ((half ^ (q >> 1)) * 64) + blk * 32 + p * 8;
  }
  const int row = 32 * wm + (l & 31);
  const unsigned ax = lbase + XBASE + row * 128 + ((h ^ ((row >> 1) & 7)) * 16);
  f32x16 acc[3] = {};
  #pragma unroll
  for (int s = 0; s < 20; ++s) {
    int rem = (19 - s) < 4 ? (19 - s) : 4;
    if (rem == 4) WAITV(12); else if (rem == 3) WAITV(9); else if (rem == 2) WAITV(6); else if (rem == 1) WAITV(3); else WAITV(0);
    __builtin_amdgcn_s_barrier();
    if (s + 5 < 20) issue_w(s + 5);
    #pragma unroll
    for (int kk = 0; kk < 2; ++kk) {
      v4u xb = *(const LAS v4u*)(size_t)((ax ^ ((kk << 5) | ((s & 1) << 6))) + (s >> 1) * 8192);
      #pragma unroll
      for (int i = 0; i < 3; ++i) {
        unsigned a = aw[i] + (s % 6) * SLOT + kk * 2048;
        s16x4 lo = __builtin_amdgcn_ds_read_tr16_b64_v4i16((LAS s16x4*)(size_t)a);
        s16x4 hi = __builtin_amdgcn_ds_read_tr16_b64_v4i16((LAS s16x4*)(size_t)(a + 512));
        bf16x8 fa = __builtin_bit_cast(bf16x8, __builtin_shufflevector(lo, hi, 0, 1, 2, 3, 4, 5, 6, 7));
        acc[i] = __builtin_amdgcn_mfma_f32_32x32x16_bf16(fa, __builtin_bit_cast(bf16x8, xb), acc[i], 0, 0, 0);
      }
    }
    WAITL(0);
  }
  const int tau = l & 31;
  #pragma unroll
  for (int i = 0; i < 3; ++i) {
    const int kb = 6 * ft + 3 * wf + i;
    const int m16 = 4 * t + 2 * wm + (tau >> 4);
    #pragma unroll
    for (int s = 0; s < 2; ++s) {
      v4u v;
      v.x = pk_relu(acc[i][8 * s + 0], acc[i][8 * s + 1]);
      v.y = pk_relu(acc[i][8 * s + 2], acc[i][8 * s + 3]);
      v.z = pk_relu(acc[i][8 * s + 4], acc[i][8 * s + 5]);
      v.w = pk_relu(acc[i][8 * s + 6], acc[i][8 * s + 7]);
      __builtin_amdgcn_raw_buffer_store_b128(v, rws, H_OFF + (m16 * 48 + kb) * 1024 + ((2 * s + h) * 16 + (tau & 15)) * 16, 0, 0);
    }
  }
}

__global__ __launch_bounds__(256, 1) void k2(const unsigned char* __restrict__ ws, unsigned char* __restrict__ out) {
  extern __shared__ __attribute__((aligned(16))) unsigned char smem[];
  const int tid = threadIdx.x, l = tid & 63, w = __builtin_amdgcn_readfirstlane(tid >> 6);
  const int b = blockIdx.x, xcd = b & 7, r = b >> 3;
  const int t = (xcd >> 1) * 8 + (r >> 2), j = (xcd & 1) * 4 + (r & 3);
  const __amdgpu_buffer_rsrc_t rws = __builtin_amdgcn_make_buffer_rsrc((void*)ws, 0, WS_BYTES, 0x00020000);
  const __amdgpu_buffer_rsrc_t ro = __builtin_amdgcn_make_buffer_rsrc((void*)out, 0, M_ * D_ * 2, 0x00020000);
  f32x4 acc[5][4] = {};
  #pragma unroll
  for (int ks = 0; ks < 12; ++ks) {
    const int kb = 12 * w + ks;
    v4u A[5], B[4];
    #pragma unroll
    for (int nt = 0; nt < 5; ++nt) A[nt] = __builtin_amdgcn_raw_buffer_load_b128(rws, W2T_OFF + ((5 * j + nt) * 48 + kb) * 1024 + l * 16, 0, 0);
    #pragma unroll
    for (int mt = 0; mt < 4; ++mt) B[mt] = __builtin_amdgcn_raw_buffer_load_b128(rws, H_OFF + ((4 * t + mt) * 48 + kb) * 1024 + l * 16, 0, 0);
    #pragma unroll
    for (int nt = 0; nt < 5; ++nt)
      #pragma unroll
      for (int mt = 0; mt < 4; ++mt)
        acc[nt][mt] = __builtin_amdgcn_mfma_f32_16x16x32_bf16(__builtin_bit_cast(bf16x8, A[nt]), __builtin_bit_cast(bf16x8, B[mt]), acc[nt][mt], 0, 0, 0);
  }
  f32x4* slab = (f32x4*)smem;
  #pragma unroll
  for (int mt = 0; mt < 4; ++mt)
    #pragma unroll
    for (int nt = 0; nt < 5; ++nt) slab[((w * 4 + mt) * 5 + nt) * 64 + l] = acc[nt][mt];
  __syncthreads();
  for (int it = tid; it < 640; it += 256) {
    const int tau = it / 10, o = it % 10;
    const int mt = tau >> 4, nt = o >> 1;
    f32x4 s0 = {0, 0, 0, 0}, s1 = {0, 0, 0, 0};
    #pragma unroll
    for (int q = 0; q < 4; ++q) {
      s0 += slab[((q * 4 + mt) * 5 + nt) * 64 + (2 * (o & 1)) * 16 + (tau & 15)];
      s1 += slab[((q * 4 + mt) * 5 + nt) * 64 + (2 * (o & 1) + 1) * 16 + (tau & 15)];
    }
    v4u v = {pk(s0.x, s0.y), pk(s0.z, s0.w), pk(s1.x, s1.y), pk(s1.z, s1.w)};
    __builtin_amdgcn_raw_buffer_store_b128(v, ro, (64 * t + tau) * 1280 + j * 160 + o * 16, 0, 0);
  }
}

extern "C" void kernel_launch(void* const* d_in, const int* in_sizes, int n_in, void* d_out, int out_size, void* d_ws, size_t ws_size,
                              hipStream_t stream) {
  hipFuncSetAttribute((const void*)k1, hipFuncAttributeMaxDynamicSharedMemorySize, LDS_BYTES);
  hipFuncSetAttribute((const void*)k2, hipFuncAttributeMaxDynamicSharedMemorySize, 81920);
  k1<<<256, 256, LDS_BYTES, stream>>>((const unsigned char*)d_in[0], (const unsigned char*)d_in[1], (const unsigned char*)d_in[2], (unsigned char*)d_ws);
  k2<<<256, 256, 81920, stream>>>((const unsigned char*)d_ws, (unsigned char*)d_out);
}
```

```cpp
#include <hip/hip_runtime.h>
#include <hip/hip_bf16.h>

constexpr unsigned LDS_BYTES = 163840;

__global__ __launch_bounds__(256, 1) void mlp_fused(const void* x, const void* w1, const void* w2, void* out, void* ws) {
  auto ka = __builtin_amdgcn_kernarg_segment_ptr();
  unsigned bid = blockIdx.x;
  unsigned tid = threadIdx.x;
  asm volatile(
    "s_load_dwordx8 s[8:15], %[ka], 0x0\n\t"
    "s_load_dwordx2 s[16:17], %[ka], 0x20\n\t"
    "v_and_b32 v1, 63, %[tid]\n\t"
    "v_lshrrev_b32 v24, 6, %[tid]\n\t"
    "s_mov_b32 s47, %[bid]\n\t"
    "s_and_b32 s54, s47, 7\n\t"
    "v_readfirstlane_b32 s44, v24\n\t"
    "s_lshr_b32 s55, s47, 3\n\t"
    "v_lshlrev_b32 v19, 4, v1\n\t"
    "v_add_u32 v44, 0x1000, v19\n\t"
    "s_getpc_b64 s[34:35]\n\t"
    "s_lshl_b32 s56, s55, 10\n\t"
    "s_cmp_lt_u32 s55, 11\n\t"
    "s_cselect_b32 s56, s56, 0\n\t"
    "s_and_b32 s34, s34, 0xffffff80\n\t"
    "v_add_u32 v31, s56, v19\n\t"
    "s_cmp_eq_u32 s44, 3\n\t"
    "s_cbranch_scc0 LNOPF%=\n\t"
    "global_load_dwordx4 v[248:251], v31, s[34:35]\n\t"
    "LNOPF%=:\n\t"
    "s_lshr_b32 s56, s54, 1\n\t"
    "s_lshl_b32 s56, s56, 3\n\t"
    "s_lshr_b32 s57, s55, 2\n\t"
    "s_add_u32 s45, s56, s57\n\t"
    "s_and_b32 s56, s54, 1\n\t"
    "s_lshl_b32 s56, s56, 2\n\t"
    "s_and_b32 s57, s55, 3\n\t"
    "s_add_u32 s46, s56, s57\n\t"
    "s_lshl_b32 s53, s44, 11\n\t"
    "v_lshrrev_b32 v24, 3, v1\n\t"
    "v_lshrrev_b32 v25, 4, v1\n\t"
    "v_and_b32 v26, 7, v1\n\t"
    "s_lshl_b32 s56, s45, 6\n\t"
    "s_lshl_b32 s57, s44, 4\n\t"
    "s_add_u32 s56, s56, s57\n\t"
    "v_add_u32 v27, s56, v24\n\t"
    "v_mul_u32_u24 v27, 0x500, v27\n\t"
    "v_xor_b32 v28, v26, v25\n\t"
    "v_lshl_add_u32 v2, v28, 4, v27\n\t"
    "v_xor_b32 v3, 64, v2\n\t"
    "v_add_u32 v3, 0x2800, v3\n\t"
    "v_add_u32 v27, s57, v24\n\t"
    "v_mul_u32_u24 v27, 0xc00, v27\n\t"
    "v_and_b32 v28, 1, v25\n\t"
    "v_lshlrev_b32 v28, 2, v28\n\t"
    "v_xor_b32 v28, v26, v28\n\t"
    "v_lshl_add_u32 v4, v28, 4, v27\n\t"
    "s_mul_i32 s56, s46, 384\n\t"
    "v_add_u32 v4, s56, v4\n\t"
    "s_mul_i32 s58, s47, 52429\n\t"
    "s_lshr_b32 s58, s58, 18\n\t"
    "s_mul_i32 s59, s58, 5\n\t"
    "s_sub_u32 s59, s47, s59\n\t"
    "s_lshl_b32 s56, s58, 5\n\t"
    "s_lshl_b32 s54, s44, 3\n\t"
    "s_add_u32 s56, s56, s54\n\t"
    "v_add_u32 v29, s56, v25\n\t"
    "v_mul_u32_u24 v29, 0x500, v29\n\t"
    "v_and_b32 v30, 15, v1\n\t"
    "v_lshl_add_u32 v29, v30, 4, v29\n\t"
    "s_lshl_b32 s57, s59, 8\n\t"
    "v_add_u32 v29, s57, v29\n\t"
    "s_add_u32 s62, s45, s46\n\t"
    "s_mul_i32 s50, s62, 52\n\t"
    "s_lshr_b32 s50, s50, 8\n\t"
    "s_mul_i32 s50, s50, 5\n\t"
    "s_sub_u32 s62, s62, s50\n\t"
    "s_mul_i32 s62, s62, 2\n\t"
    "s_lshl_b32 s50, s62, 7\n\t"
    "s_mul_i32 s51, s62, 196608\n\t"
    "s_mov_b32 s75, s51\n\t"
    "s_add_u32 s52, s51, 24576\n\t"
    "v_mov_b32 v36, 0\n\t"
    "s_waitcnt lgkmcnt(0)\n\t"
    "s_mov_b32 s36, s16\n\t"
    "s_and_b32 s37, s17, 0xffff\n\t"
    "s_mov_b32 s38, 8355840\n\t"
    "s_mov_b32 s39, 0x20000\n\t"
    "s_mov_b32 s28, s12\n\t"
    "s_and_b32 s29, s13, 0xffff\n\t"
    "s_mov_b32 s30, 1966080\n\t"
    "s_mov_b32 s31, 0x20000\n\t"
    "buffer_load_dwordx2 v[32:33], v36, s[36:39], 0 offen sc1\n\t"
    "s_add_u32 m0, s53, 147456\n\t"
    "s_movk_i32 s54, 0x1400\n\t"
    "buffer_load_dwordx4 v29, s[28:31], 0 offen lds\n\t"
    "s_add_u32 m0, s53, 148480\n\t"
    "s_mov_b32 s20, s8\n\t"
    "s_and_b32 s21, s9, 0xffff\n\t"
    "s_mov_b32 s22, 2621440\n\t"
    "s_mov_b32 s23, 0x20000\n\t"
    "buffer_load_dwordx4 v29, s[28:31], s54 offen lds\n\t"
    "s_mov_b32 s24, s10\n\t"
    "s_and_b32 s25, s11, 0xffff\n\t"
    "s_mov_b32 s26, 1966080\n\t"
    "s_mov_b32 s27, 0x20000\n\t"
    "s_add_u32 m0, s53, 0\n\t"
    "s_nop 0\n\t"
    "buffer_load_dwordx4 v2, s[20:23], s50 offen lds\n\t"
    "s_mov_b32 s40, s14\n\t"
    "s_and_b32 s41, s15, 0xffff\n\t"
    "s_mov_b32 s42, 2621440\n\t"
    "s_mov_b32 s43, 0x20000\n\t"
    "s_lshr_b32 s54, s44, 1\n\t"
    "s_lshl_b32 s54, s54, 12\n\t"
    "v_and_b32 v24, 31, v1\n\t"
    "s_add_u32 m0, s53, 1024\n\t"
    "s_nop 0\n\t"
    "buffer_load_dwordx4 v3, s[20:23], s50 offen lds\n\t"
    "v_lshlrev_b32 v24, 7, v24\n\t"
    "v_add_u32 v24, s54, v24\n\t"
    "v_lshrrev_b32 v25, 1, v1\n\t"
    "v_and_b32 v25, 7, v25\n\t"
    "v_lshrrev_b32 v26, 5, v1\n\t"
    "v_xor_b32 v25, v25, v26\n\t"
    "v_lshl_add_u32 v5, v25, 4, v24\n\t"
    "s_add_u32 m0, s53, 8192\n\t"
    "s_nop 0\n\t"
    "buffer_load_dwordx4 v4, s[24:27], s51 offen lds\n\t"
    "v_xor_b32 v6, 32, v5\n\t"
    "v_xor_b32 v7, 64, v5\n\t"
    "v_xor_b32 v8, 96, v5\n\t"
    "v_bfe_u32 v24, v1, 2, 2\n\t"
    "v_lshlrev_b32 v25, 3, v26\n\t"
    "v_add_u32 v25, v25, v24\n\t"
    "v_lshlrev_b32 v25, 7, v25\n\t"
    "s_add_u32 m0, s53, 16256\n\t"
    "s_nop 0\n\t"
    "buffer_load_dwordx4 v4, s[24:27], s51 offen offset:128 lds\n\t"
    "v_lshrrev_b32 v27, 1, v24\n\t"
    "v_lshl_add_u32 v25, v27, 6, v25\n\t"
    "v_bfe_u32 v27, v1, 4, 1\n\t"
    "v_lshl_add_u32 v25, v27, 5, v25\n\t"
    "v_and_b32 v27, 3, v1\n\t"
    "v_lshl_add_u32 v25, v27, 3, v25\n\t"
    "s_and_b32 s54, s44, 1\n\t"
    "s_add_u32 m0, s53, 9216\n\t"
    "s_nop 0\n\t"
    "buffer_load_dwordx4 v4, s[24:27], s52 offen lds\n\t"
    "s_lshl_b32 s55, s54, 13\n\t"
    "s_add_u32 s55, s55, 8192\n\t"
    "v_add_u32 v9, s55, v25\n\t"
    "v_xor_b32 v10, 64, v9\n\t"
    "s_lshl_b32 s55, s54, 6\n\t"
    "v_xor_b32 v11, s55, v25\n\t"
    "v_add_u32 v11, 0x2000, v11\n\t"
    "s_add_u32 m0, s53, 17280\n\t"
    "s_nop 0\n\t"
    "buffer_load_dwordx4 v4, s[24:27], s52 offen offset:128 lds\n\t"
    "v_add_u32 v160, 49152, v5\n\t"
    "v_add_u32 v161, 49152, v6\n\t"
    "v_add_u32 v162, 49152, v7\n\t"
    "v_add_u32 v163, 49152, v8\n\t"
    "v_add_u32 v164, 49152, v9\n\t"
    "v_add_u32 v165, 49152, v10\n\t"
    "v_add_u32 v166, 49152, v11\n\t"
    "s_add_u32 s50, s50, 128\n\t"
    "s_cmp_eq_u32 s50, 1280\n\t"
    "s_cselect_b32 s50, 0, s50\n\t"
    "s_add_u32 s51, s51, 196608\n\t"
    "s_cmp_eq_u32 s51, 1966080\n\t"
    "s_cselect_b32 s51, 0, s51\n\t"
    "s_add_u32 s52, s51, 24576\n\t"
    "v_add_u32 v167, 98304, v5\n\t"
    "v_add_u32 v168, 98304, v6\n\t"
    "v_add_u32 v169, 98304, v7\n\t"
    "v_add_u32 v170, 98304, v8\n\t"
    "v_add_u32 v171, 98304, v9\n\t"
    "v_add_u32 v172, 98304, v10\n\t"
    "v_add_u32 v173, 98304, v11\n\t"
    "s_add_u32 m0, s53, 24576\n\t"
    "s_nop 0\n\t"
    "buffer_load_dwordx4 v2, s[20:23], s50 offen lds\n\t"
    "v_bfe_u32 v24, v1, 4, 1\n\t"
    "v_mul_u32_u24 v24, 12288, v24\n\t"
    "v_lshl_add_u32 v24, v26, 8, v24\n\t"
    "v_and_b32 v25, 15, v1\n\t"
    "v_lshl_add_u32 v20, v25, 4, v24\n\t"
    "s_lshl_b32 s56, s45, 2\n\t"
    "s_lshr_b32 s57, s46, 1\n\t"
    "s_add_u32 m0, s53, 25600\n\t"
    "s_nop 0\n\t"
    "buffer_load_dwordx4 v3, s[20:23], s50 offen lds\n\t"
    "s_add_u32 s56, s56, s57\n\t"
    "s_lshl_b32 s56, s56, 2\n\t"
    "s_and_b32 s57, s44, 2\n\t"
    "s_add_u32 s56, s56, s57\n\t"
    "s_mul_i32 s56, s56, 12\n\t"
    "s_and_b32 s57, s46, 1\n\t"
    "s_mul_i32 s57, s57, 6\n\t"
    "s_add_u32 m0, s53, 32768\n\t"
    "s_nop 0\n\t"
    "buffer_load_dwordx4 v4, s[24:27], s51 offen lds\n\t"
    "s_add_u32 s56, s56, s57\n\t"
    "s_lshl_b32 s57, s54, 1\n\t"
    "s_add_u32 s63, s56, s57\n\t"
    "s_lshl_b32 s63, s63, 10\n\t"
    "s_add_u32 s63, s63, 32768\n\t"
    "s_add_u32 s67, s56, s54\n\t"
    "s_add_u32 s67, s67, 4\n\t"
    "s_add_u32 m0, s53, 40832\n\t"
    "s_nop 0\n\t"
    "buffer_load_dwordx4 v4, s[24:27], s51 offen offset:128 lds\n\t"
    "s_lshl_b32 s67, s67, 10\n\t"
    "s_add_u32 s67, s67, 32768\n\t"
    "s_add_u32 s54, s45, s46\n\t"
    "s_and_b32 s54, s54, 1\n\t"
    "s_mul_i32 s54, s54, 6\n\t"
    "s_lshl_b32 s56, s46, 2\n\t"
    "s_add_u32 s56, s56, s44\n\t"
    "s_add_u32 m0, s53, 33792\n\t"
    "s_nop 0\n\t"
    "buffer_load_dwordx4 v4, s[24:27], s52 offen lds\n\t"
    "s_mul_i32 s56, s56, 61440\n\t"
    "s_add_u32 s56, s56, 6324224\n\t"
    "s_mul_i32 s57, s54, 5120\n\t"
    "s_add_u32 s64, s56, s57\n\t"
    "s_sub_u32 s72, s56, s57\n\t"
    "s_lshl_b32 s56, s45, 2\n\t"
    "s_add_u32 s56, s56, s44\n\t"
    "s_add_u32 m0, s53, 41856\n\t"
    "s_nop 0\n\t"
    "buffer_load_dwordx4 v4, s[24:27], s52 offen offset:128 lds\n\t"
    "s_mul_i32 s56, s56, 49152\n\t"
    "s_add_u32 s56, s56, 32768\n\t"
    "s_lshl_b32 s57, s54, 10\n\t"
    "s_add_u32 s65, s56, s57\n\t"
    "s_sub_u32 s73, s56, s57\n\t"
    "s_add_u32 s56, s44, 0\n\t"
    "s_and_b32 s56, s56, 3\n\t"
    "s_add_u32 s50, s50, 128\n\t"
    "s_cmp_eq_u32 s50, 1280\n\t"
    "s_cselect_b32 s50, 0, s50\n\t"
    "s_add_u32 s51, s51, 196608\n\t"
    "s_cmp_eq_u32 s51, 1966080\n\t"
    "s_cselect_b32 s51, 0, s51\n\t"
    "s_add_u32 s52, s51, 24576\n\t"
    "s_lshl_b32 s56, s56, 10\n\t"
    "v_add_u32 v40, s56, v19\n\t"
    "s_add_u32 s56, s44, 1\n\t"
    "s_and_b32 s56, s56, 3\n\t"
    "s_lshl_b32 s56, s56, 10\n\t"
    "v_add_u32 v41, s56, v19\n\t"
    "s_add_u32 s56, s44, 2\n\t"
    "s_add_u32 m0, s53, 49152\n\t"
    "s_nop 0\n\t"
    "buffer_load_dwordx4 v2, s[20:23], s50 offen lds\n\t"
    "s_and_b32 s56, s56, 3\n\t"
    "s_lshl_b32 s56, s56, 10\n\t"
    "v_add_u32 v42, s56, v19\n\t"
    "s_add_u32 s56, s44, 3\n\t"
    "s_and_b32 s56, s56, 3\n\t"
    "s_lshl_b32 s56, s56, 10\n\t"
    "v_add_u32 v43, s56, v19\n\t"
    "s_add_u32 m0, s53, 50176\n\t"
    "s_nop 0\n\t"
    "buffer_load_dwordx4 v3, s[20:23], s50 offen lds\n\t"
    "v_min_u32 v24, 35, v1\n\t"
    "v_mul_u32_u24 v25, 43, v24\n\t"
    "v_lshrrev_b32 v25, 7, v25\n\t"
    "v_mul_u32_u24 v27, 3, v25\n\t"
    "v_sub_u32 v24, v24, v27\n\t"
    "v_mul_u32_u24 v25, 20, v25\n\t"
    "v_add_u32 v24, v24, v25\n\t"
    "s_add_u32 m0, s53, 57344\n\t"
    "s_nop 0\n\t"
    "buffer_load_dwordx4 v4, s[24:27], s51 offen lds\n\t"
    "s_mul_i32 s56, s44, 240\n\t"
    "s_mul_i32 s57, s46, 5\n\t"
    "s_lshr_b32 s57, s57, 1\n\t"
    "s_add_u32 s56, s56, s57\n\t"
    "v_add_u32 v24, s56, v24\n\t"
    "v_lshlrev_b32 v24, 3, v24\n\t"
    "v_add_u32 v21, 16384, v24\n\t"
    "s_add_u32 m0, s53, 65408\n\t"
    "s_nop 0\n\t"
    "buffer_load_dwordx4 v4, s[24:27], s51 offen offset:128 lds\n\t"
    "s_lshl_b32 s56, s45, 5\n\t"
    "s_lshl_b32 s57, s44, 3\n\t"
    "s_add_u32 s56, s56, s57\n\t"
    "v_and_b32 v24, 7, v1\n\t"
    "v_add_u32 v24, s56, v24\n\t"
    "v_lshlrev_b32 v22, 3, v24\n\t"
    "v_add_u32 v16, 8192, v22\n\t"
    "s_add_u32 m0, s53, 58368\n\t"
    "s_nop 0\n\t"
    "buffer_load_dwordx4 v4, s[24:27], s52 offen lds\n\t"
    "s_lshl_b32 s56, s45, 3\n\t"
    "s_add_u32 s56, s56, s46\n\t"
    "s_lshl_b32 s56, s56, 2\n\t"
    "s_add_u32 s56, s56, s44\n\t"
    "s_lshl_b32 s74, s56, 3\n\t"
    "v_lshrrev_b32 v24, 5, v1\n\t"
    "v_lshlrev_b32 v24, 4, v24\n\t"
    "s_add_u32 m0, s53, 66432\n\t"
    "s_nop 0\n\t"
    "buffer_load_dwordx4 v4, s[24:27], s52 offen offset:128 lds\n\t"
    "v_bfe_u32 v25, v1, 4, 1\n\t"
    "v_lshl_add_u32 v24, v25, 2, v24\n\t"
    "v_bfe_u32 v25, v1, 2, 2\n\t"
    "v_add_u32 v24, v24, v25\n\t"
    "v_lshlrev_b32 v24, 8, v24\n\t"
    "v_and_b32 v25, 3, v1\n\t"
    "v_lshl_add_u32 v24, v25, 3, v24\n\t"
    "s_add_u32 s50, s50, 128\n\t"
    "s_cmp_eq_u32 s50, 1280\n\t"
    "s_cselect_b32 s50, 0, s50\n\t"
    "s_add_u32 s51, s51, 196608\n\t"
    "s_cmp_eq_u32 s51, 1966080\n\t"
    "s_cselect_b32 s51, 0, s51\n\t"
    "s_add_u32 s52, s51, 24576\n\t"
    "s_lshl_b32 s56, s44, 6\n\t"
    "s_add_u32 s56, s56, 147456\n\t"
    "v_add_u32 v28, s56, v24\n\t"
    "s_mul_i32 s54, s58, 43\n\t"
    "s_lshr_b32 s54, s54, 9\n\t"
    "s_mul_i32 s55, s54, 12\n\t"
    "s_sub_u32 s55, s58, s55\n\t"
    "s_add_u32 m0, s53, 73728\n\t"
    "s_nop 0\n\t"
    "buffer_load_dwordx4 v2, s[20:23], s50 offen lds\n\t"
    "s_mul_i32 s55, s55, 5\n\t"
    "s_lshl_b32 s56, s59, 3\n\t"
    "s_lshl_b32 s57, s44, 1\n\t"
    "s_add_u32 s56, s56, s57\n\t"
    "s_mul_i32 s57, s56, 52\n\t"
    "s_lshr_b32 s57, s57, 8\n\t"
    "s_mul_i32 s60, s57, 5\n\t"
    "s_add_u32 m0, s53, 74752\n\t"
    "s_nop 0\n\t"
    "buffer_load_dwordx4 v3, s[20:23], s50 offen lds\n\t"
    "s_sub_u32 s56, s56, s60\n\t"
    "s_lshl_b32 s57, s57, 2\n\t"
    "s_add_u32 s57, s57, s54\n\t"
    "s_mul_i32 s57, s57, 60\n\t"
    "s_add_u32 s57, s57, s55\n\t"
    "s_add_u32 s57, s57, s56\n\t"
    "s_lshl_b32 s57, s57, 10\n\t"
    "s_add_u32 m0, s53, 81920\n\t"
    "s_nop 0\n\t"
    "buffer_load_dwordx4 v4, s[24:27], s51 offen lds\n\t"
    "s_add_u32 s70, s57, 6324224\n\t"
    "s_lshl_b32 s56, s59, 3\n\t"
    "s_lshl_b32 s57, s44, 1\n\t"
    "s_add_u32 s56, s56, s57\n\t"
    "s_add_u32 s56, s56, 1\n\t"
    "s_mul_i32 s57, s56, 52\n\t"
    "s_lshr_b32 s57, s57, 8\n\t"
    "s_add_u32 m0, s53, 89984\n\t"
    "s_nop 0\n\t"
    "buffer_load_dwordx4 v4, s[24:27], s51 offen offset:128 lds\n\t"
    "s_mul_i32 s60, s57, 5\n\t"
    "s_sub_u32 s56, s56, s60\n\t"
    "s_lshl_b32 s57, s57, 2\n\t"
    "s_add_u32 s57, s57, s54\n\t"
    "s_mul_i32 s57, s57, 60\n\t"
    "s_add_u32 s57, s57, s55\n\t"
    "s_add_u32 s57, s57, s56\n\t"
    "s_add_u32 m0, s53, 82944\n\t"
    "s_nop 0\n\t"
    "buffer_load_dwordx4 v4, s[24:27], s52 offen lds\n\t"
    "s_lshl_b32 s57, s57, 10\n\t"
    "s_add_u32 s71, s57, 6324224\n\t"
    "s_mul_i32 s56, s58, 20\n\t"
    "s_lshl_b32 s57, s59, 2\n\t"
    "s_add_u32 s56, s56, s57\n\t"
    "s_add_u32 s56, s56, s44\n\t"
    "s_lshl_b32 s56, s56, 3\n\t"
    "s_add_u32 m0, s53, 91008\n\t"
    "s_nop 0\n\t"
    "buffer_load_dwordx4 v4, s[24:27], s52 offen offset:128 lds\n\t"
    "s_add_u32 s18, s56, 16384\n\t"
    "s_cmp_lt_u32 s47, 240\n\t"
    "s_cselect_b32 s56, 0, 0x7ff00000\n\t"
    "s_cselect_b32 s70, s70, 0\n\t"
    "s_cselect_b32 s71, s71, 0\n\t"
    "s_cselect_b32 s18, s18, 0\n\t"
    "v_add_u32 v37, s56, v19\n\t"
    "v_mov_b32 v23, s56\n\t"
    "s_add_u32 s50, s50, 128\n\t"
    "s_cmp_eq_u32 s50, 1280\n\t"
    "s_cselect_b32 s50, 0, s50\n\t"
    "s_add_u32 s51, s51, 196608\n\t"
    "s_cmp_eq_u32 s51, 1966080\n\t"
    "s_cselect_b32 s51, 0, s51\n\t"
    "s_add_u32 s52, s51, 24576\n\t"
    "s_waitcnt vmcnt(18)\n\t"
    "s_barrier\n\t"
    "v_readfirstlane_b32 s54, v32\n\t"
    "v_readfirstlane_b32 s55, v33\n\t"
    "ds_read_b64_tr_b16 v[224:225], v28 offset:0\n\t"
    "ds_read_b64_tr_b16 v[226:227], v28 offset:2048\n\t"
    "ds_read_b64_tr_b16 v[228:229], v28 offset:32\n\t"
    "ds_read_b64_tr_b16 v[230:231], v28 offset:2080\n\t"
    "ds_read_b128 a[0:3], v5\n\t"
    "ds_read_b64_tr_b16 v[96:97], v9 offset:0\n\t"
    "ds_read_b64_tr_b16 v[98:99], v9 offset:512\n\t"
    "ds_read_b64_tr_b16 v[100:101], v10 offset:0\n\t"
    "ds_read_b64_tr_b16 v[102:103], v10 offset:512\n\t"
    "ds_read_b128 a[4:7], v6\n\t"
    "ds_read_b64_tr_b16 v[104:105], v9 offset:2048\n\t"
    "ds_read_b64_tr_b16 v[106:107], v9 offset:2560\n\t"
    "ds_read_b64_tr_b16 v[108:109], v10 offset:2048\n\t"
    "ds_read_b64_tr_b16 v[110:111], v10 offset:2560\n\t"
    "ds_read_b128 a[8:11], v7\n\t"
    "ds_read_b64_tr_b16 v[112:113], v9 offset:4096\n\t"
    "ds_read_b64_tr_b16 v[114:115], v9 offset:4608\n\t"
    "ds_read_b64_tr_b16 v[116:117], v10 offset:4096\n\t"
    "ds_read_b64_tr_b16 v[118:119], v10 offset:4608\n\t"
    "ds_read_b128 a[12:15], v8\n\t"
    "ds_read_b64_tr_b16 v[120:121], v9 offset:6144\n\t"
    "ds_read_b64_tr_b16 v[122:123], v9 offset:6656\n\t"
    "ds_read_b64_tr_b16 v[124:125], v10 offset:6144\n\t"
    "ds_read_b64_tr_b16 v[126:127], v10 offset:6656\n\t"
    "s_mul_i32 s48, s54, 0x9E3779B1\n\t"
    "s_add_u32 s48, s48, 0x7F4A7C15\n\t"
    "s_add_u32 s49, s55, 1\n\t"
    "s_mov_b32 s19, 65536\n\t"
    "s_add_u32 s70, s70, s19\n\t"
    "s_add_u32 s71, s71, s19\n\t"
    "s_add_u32 s63, s63, s19\n\t"
    "s_add_u32 s67, s67, s19\n\t"
    "s_add_u32 s64, s64, s19\n\t"
    "s_add_u32 s72, s72, s19\n\t"
    "s_add_u32 s65, s65, s19\n\t"
    "s_add_u32 s73, s73, s19\n\t"
    "s_add_u32 s18, s18, s19\n\t"
    "s_add_u32 s74, s74, s19\n\t"
    "v_mov_b32 v34, s48\n\t"
    "v_mov_b32 v35, s49\n\t"
    "s_waitcnt lgkmcnt(0)\n\t"
    "buffer_store_dwordx4 v[224:227], v37, s[36:39], s70 offen sc1\n\t"
    "buffer_store_dwordx4 v[228:231], v37, s[36:39], s71 offen sc1\n\t"
    "s_waitcnt vmcnt(14) lgkmcnt(0)\n\t"
    "s_barrier\n\t"
    "s_add_u32 m0, s53, 98304\n\t"
    "s_nop 0\n\t"
    "buffer_load_dwordx4 v2, s[20:23], s50 offen lds\n\t"
    "s_add_u32 m0, s53, 99328\n\t"
    "s_nop 0\n\t"
    "buffer_load_dwordx4 v3, s[20:23], s50 offen lds\n\t"
    "s_add_u32 m0, s53, 106496\n\t"
    "s_nop 0\n\t"
    "buffer_load_dwordx4 v4, s[24:27], s51 offen lds\n\t"
    "ds_read_b128 a[16:19], v5 offset:24576\n\t"
    "ds_read_b64_tr_b16 v[128:129], v9 offset:24576\n\t"
    "ds_read_b64_tr_b16 v[130:131], v9 offset:25088\n\t"
    "ds_read_b64_tr_b16 v[132:133], v10 offset:24576\n\t"
    "ds_read_b64_tr_b16 v[134:135], v10 offset:25088\n\t"
    "s_add_u32 m0, s53, 114560\n\t"
    "v_mfma_f32_32x32x16_bf16 v[48:63], v[96:99], a[0:3], 0\n\t"
    "buffer_load_dwordx4 v4, s[24:27], s51 offen offset:128 lds\n\t"
    "s_add_u32 m0, s53, 107520\n\t"
    "s_nop 0\n\t"
    "buffer_load_dwordx4 v4, s[24:27], s52 offen lds\n\t"
    "ds_read_b128 a[20:23], v6 offset:24576\n\t"
    "ds_read_b64_tr_b16 v[136:137], v9 offset:26624\n\t"
    "ds_read_b64_tr_b16 v[138:139], v9 offset:27136\n\t"
    "ds_read_b64_tr_b16 v[140:141], v10 offset:26624\n\t"
    "ds_read_b64_tr_b16 v[142:143], v10 offset:27136\n\t"
    "s_add_u32 m0, s53, 115584\n\t"
    "v_mfma_f32_32x32x16_bf16 v[64:79], v[100:103], a[0:3], 0\n\t"
    "buffer_load_dwordx4 v4, s[24:27], s52 offen offset:128 lds\n\t"
    "s_add_u32 s50, s50, 128\n\t"
    "s_cmp_eq_u32 s50, 1280\n\t"
    "s_cselect_b32 s50, 0, s50\n\t"
    "s_add_u32 s51, s51, 196608\n\t"
    "s_cmp_eq_u32 s51, 1966080\n\t"
    "s_cselect_b32 s51, 0, s51\n\t"
    "s_add_u32 s52, s51, 24576\n\t"
    "ds_read_b128 a[24:27], v7 offset:24576\n\t"
    "ds_read_b64_tr_b16 v[144:145], v9 offset:28672\n\t"
    "ds_read_b64_tr_b16 v[146:147], v9 offset:29184\n\t"
    "ds_read_b64_tr_b16 v[148:149], v10 offset:28672\n\t"
    "ds_read_b64_tr_b16 v[150:151], v10 offset:29184\n\t"
    "s_add_u32 m0, s53, 122880\n\t"
    "v_mfma_f32_32x32x16_bf16 v[48:63], v[104:107], a[4:7], v[48:63]\n\t"
    "buffer_load_dwordx4 v2, s[20:23], s50 offen lds\n\t"
    "s_add_u32 m0, s53, 123904\n\t"
    "s_nop 0\n\t"
    "buffer_load_dwordx4 v3, s[20:23], s50 offen lds\n\t"
    "s_add_u32 m0, s53, 131072\n\t"
    "s_nop 0\n\t"
    "buffer_load_dwordx4 v4, s[24:27], s51 offen lds\n\t"
    "ds_read_b128 a[28:31], v8 offset:24576\n\t"
    "ds_read_b64_tr_b16 v[152:153], v9 offset:30720\n\t"
    "ds_read_b64_tr_b16 v[154:155], v9 offset:31232\n\t"
    "ds_read_b64_tr_b16 v[156:157], v10 offset:30720\n\t"
    "ds_read_b64_tr_b16 v[158:159], v10 offset:31232\n\t"
    "s_add_u32 m0, s53, 139136\n\t"
    "v_mfma_f32_32x32x16_bf16 v[64:79], v[108:111], a[4:7], v[64:79]\n\t"
    "buffer_load_dwordx4 v4, s[24:27], s51 offen offset:128 lds\n\t"
    "s_add_u32 m0, s53, 132096\n\t"
    "s_nop 0\n\t"
    "buffer_load_dwordx4 v4, s[24:27], s52 offen lds\n\t"
    "s_add_u32 m0, s53, 140160\n\t"
    "v_mfma_f32_32x32x16_bf16 v[48:63], v[112:115], a[8:11], v[48:63]\n\t"
    "buffer_load_dwordx4 v4, s[24:27], s52 offen offset:128 lds\n\t"
    "s_add_u32 s50, s50, 128\n\t"
    "s_cmp_eq_u32 s50, 1280\n\t"
    "s_cselect_b32 s50, 0, s50\n\t"
    "s_add_u32 s51, s51, 196608\n\t"
    "s_cmp_eq_u32 s51, 1966080\n\t"
    "s_cselect_b32 s51, 0, s51\n\t"
    "s_add_u32 s52, s51, 24576\n\t"
    "v_mfma_f32_32x32x16_bf16 v[64:79], v[116:119], a[8:11], v[64:79]\n\t"
    "s_waitcnt vmcnt(20) lgkmcnt(0)\n\t"
    "s_barrier\n\t"
    "s_add_u32 m0, s53, 0\n\t"
    "s_nop 0\n\t"
    "buffer_load_dwordx4 v2, s[20:23], s50 offen lds\n\t"
    "ds_read_b128 a[32:35], v160\n\t"
    "ds_read_b64_tr_b16 v[96:97], v164 offset:0\n\t"
    "ds_read_b64_tr_b16 v[98:99], v164 offset:512\n\t"
    "ds_read_b64_tr_b16 v[100:101], v165 offset:0\n\t"
    "s_add_u32 m0, s53, 1024\n\t"
    "v_mfma_f32_32x32x16_bf16 v[48:63], v[120:123], a[12:15], v[48:63]\n\t"
    "buffer_load_dwordx4 v3, s[20:23], s50 offen lds\n\t"
    "ds_read_b64_tr_b16 v[102:103], v165 offset:512\n\t"
    "ds_read_b128 a[36:39], v161\n\t"
    "ds_read_b64_tr_b16 v[104:105], v164 offset:2048\n\t"
    "s_add_u32 m0, s53, 8192\n\t"
    "v_mfma_f32_32x32x16_bf16 v[64:79], v[124:127], a[12:15], v[64:79]\n\t"
    "buffer_load_dwordx4 v4, s[24:27], s51 offen lds\n\t"
    "ds_read_b64_tr_b16 v[106:107], v164 offset:2560\n\t"
    "ds_read_b64_tr_b16 v[108:109], v165 offset:2048\n\t"
    "ds_read_b64_tr_b16 v[110:111], v165 offset:2560\n\t"
    "s_add_u32 m0, s53, 16256\n\t"
    "v_mfma_f32_32x32x16_bf16 v[48:63], v[128:131], a[16:19], v[48:63]\n\t"
    "buffer_load_dwordx4 v4, s[24:27], s51 offen offset:128 lds\n\t"
    "ds_read_b128 a[40:43], v162\n\t"
    "ds_read_b64_tr_b16 v[112:113], v164 offset:4096\n\t"
    "ds_read_b64_tr_b16 v[114:115], v164 offset:4608\n\t"
    "ds_read_b64_tr_b16 v[116:117], v165 offset:4096\n\t"
    "s_add_u32 m0, s53, 9216\n\t"
    "v_mfma_f32_32x32x16_bf16 v[64:79], v[132:135], a[16:19], v[64:79]\n\t"
    "buffer_load_dwordx4 v4, s[24:27], s52 offen lds\n\t"
    "ds_read_b64_tr_b16 v[118:119], v165 offset:4608\n\t"
    "ds_read_b128 a[44:47], v163\n\t"
    "ds_read_b64_tr_b16 v[120:121], v164 offset:6144\n\t"
    "s_add_u32 m0, s53, 17280\n\t"
    "v_mfma_f32_32x32x16_bf16 v[48:63], v[136:139], a[20:23], v[48:63]\n\t"
    "buffer_load_dwordx4 v4, s[24:27], s52 offen offset:128 lds\n\t"
    "ds_read_b64_tr_b16 v[122:123], v164 offset:6656\n\t"
    "ds_read_b64_tr_b16 v[124:125], v165 offset:6144\n\t"
    "ds_read_b64_tr_b16 v[126:127], v165 offset:6656\n\t"
    "v_mfma_f32_32x32x16_bf16 v[64:79], v[140:143], a[20:23], v[64:79]\n\t"
    "s_add_u32 s50, s50, 128\n\t"
    "s_cmp_eq_u32 s50, 1280\n\t"
    "s_cselect_b32 s50, 0, s50\n\t"
    "s_add_u32 s51, s51, 196608\n\t"
    "s_cmp_eq_u32 s51, 1966080\n\t"
    "s_cselect_b32 s51, 0, s51\n\t"
    "s_add_u32 s52, s51, 24576\n\t"
    "v_mfma_f32_32x32x16_bf16 v[48:63], v[144:147], a[24:27], v[48:63]\n\t"
    "v_mfma_f32_32x32x16_bf16 v[64:79], v[148:151], a[24:27], v[64:79]\n\t"
    "s_waitcnt vmcnt(20) lgkmcnt(0)\n\t"
    "s_barrier\n\t"
    "s_waitcnt vmcnt(18)\n\t"
    "s_mov_b64 s[60:61], exec\n\t"
    "s_mov_b64 exec, 1\n\t"
    "buffer_store_dwordx2 v[34:35], v23, s[36:39], s18 offen sc1\n\t"
    "s_mov_b64 exec, s[60:61]\n\t"
    "s_add_u32 m0, s53, 24576\n\t"
    "s_nop 0\n\t"
    "buffer_load_dwordx4 v2, s[20:23], s50 offen lds\n\t"
    "ds_read_b128 a[48:51], v160 offset:24576\n\t"
    "ds_read_b64_tr_b16 v[128:129], v164 offset:24576\n\t"
    "ds_read_b64_tr_b16 v[130:131], v164 offset:25088\n\t"
    "ds_read_b64_tr_b16 v[132:133], v165 offset:24576\n\t"
    "s_add_u32 m0, s53, 25600\n\t"
    "v_mfma_f32_32x32x16_bf16 v[48:63], v[152:155], a[28:31], v[48:63]\n\t"
    "buffer_load_dwordx4 v3, s[20:23], s50 offen lds\n\t"
    "ds_read_b64_tr_b16 v[134:135], v165 offset:25088\n\t"
    "ds_read_b128 a[52:55], v161 offset:24576\n\t"
    "ds_read_b64_tr_b16 v[136:137], v164 offset:26624\n\t"
    "s_add_u32 m0, s53, 32768\n\t"
    "v_mfma_f32_32x32x16_bf16 v[64:79], v[156:159], a[28:31], v[64:79]\n\t"
    "buffer_load_dwordx4 v4, s[24:27], s51 offen lds\n\t"
    "ds_read_b64_tr_b16 v[138:139], v164 offset:27136\n\t"
    "ds_read_b64_tr_b16 v[140:141], v165 offset:26624\n\t"
    "ds_read_b64_tr_b16 v[142:143], v165 offset:27136\n\t"
    "s_add_u32 m0, s53, 40832\n\t"
    "v_mfma_f32_32x32x16_bf16 v[48:63], v[96:99], a[32:35], v[48:63]\n\t"
    "buffer_load_dwordx4 v4, s[24:27], s51 offen offset:128 lds\n\t"
    "ds_read_b128 a[56:59], v162 offset:24576\n\t"
    "ds_read_b64_tr_b16 v[144:145], v164 offset:28672\n\t"
    "ds_read_b64_tr_b16 v[146:147], v164 offset:29184\n\t"
    "ds_read_b64_tr_b16 v[148:149], v165 offset:28672\n\t"
    "s_add_u32 m0, s53, 33792\n\t"
    "v_mfma_f32_32x32x16_bf16 v[64:79], v[100:103], a[32:35], v[64:79]\n\t"
    "buffer_load_dwordx4 v4, s[24:27], s52 offen lds\n\t"
    "ds_read_b64_tr_b16 v[150:151], v165 offset:29184\n\t"
    "ds_read_b128 a[60:63], v163 offset:24576\n\t"
    "ds_read_b64_tr_b16 v[152:153], v164 offset:30720\n\t"
    "s_add_u32 m0, s53, 41856\n\t"
    "v_mfma_f32_32x32x16_bf16 v[48:63], v[104:107], a[36:39], v[48:63]\n\t"
    "buffer_load_dwordx4 v4, s[24:27], s52 offen offset:128 lds\n\t"
    "ds_read_b64_tr_b16 v[154:155], v164 offset:31232\n\t"
    "ds_read_b64_tr_b16 v[156:157], v165 offset:30720\n\t"
    "ds_read_b64_tr_b16 v[158:159], v165 offset:31232\n\t"
    "v_mfma_f32_32x32x16_bf16 v[64:79], v[108:111], a[36:39], v[64:79]\n\t"
    "s_add_u32 s50, s50, 128\n\t"
    "s_cmp_eq_u32 s50, 1280\n\t"
    "s_cselect_b32 s50, 0, s50\n\t"
    "s_add_u32 s51, s51, 196608\n\t"
    "s_cmp_eq_u32 s51, 1966080\n\t"
    "s_cselect_b32 s51, 0, s51\n\t"
    "s_add_u32 s52, s51, 24576\n\t"
    "v_mfma_f32_32x32x16_bf16 v[48:63], v[112:115], a[40:43], v[48:63]\n\t"
    "v_mfma_f32_32x32x16_bf16 v[64:79], v[116:119], a[40:43], v[64:79]\n\t"
    "s_waitcnt vmcnt(19) lgkmcnt(0)\n\t"
    "s_barrier\n\t"
    "s_add_u32 m0, s53, 49152\n\t"
    "s_nop 0\n\t"
    "buffer_load_dwordx4 v2, s[20:23], s50 offen lds\n\t"
    "ds_read_b128 a[64:67], v167\n\t"
    "ds_read_b64_tr_b16 v[96:97], v171 offset:0\n\t"
    "ds_read_b64_tr_b16 v[98:99], v171 offset:512\n\t"
    "ds_read_b64_tr_b16 v[100:101], v172 offset:0\n\t"
    "s_add_u32 m0, s53, 50176\n\t"
    "v_mfma_f32_32x32x16_bf16 v[48:63], v[120:123], a[44:47], v[48:63]\n\t"
    "buffer_load_dwordx4 v3, s[20:23], s50 offen lds\n\t"
    "ds_read_b64_tr_b16 v[102:103], v172 offset:512\n\t"
    "ds_read_b128 a[68:71], v168\n\t"
    "ds_read_b64_tr_b16 v[104:105], v171 offset:2048\n\t"
    "s_add_u32 m0, s53, 57344\n\t"
    "v_mfma_f32_32x32x16_bf16 v[64:79], v[124:127], a[44:47], v[64:79]\n\t"
    "buffer_load_dwordx4 v4, s[24:27], s51 offen lds\n\t"
    "ds_read_b64_tr_b16 v[106:107], v171 offset:2560\n\t"
    "ds_read_b64_tr_b16 v[108:109], v172 offset:2048\n\t"
    "ds_read_b64_tr_b16 v[110:111], v172 offset:2560\n\t"
    "s_add_u32 m0, s53, 65408\n\t"
    "v_mfma_f32_32x32x16_bf16 v[48:63], v[128:131], a[48:51], v[48:63]\n\t"
    "buffer_load_dwordx4 v4, s[24:27], s51 offen offset:128 lds\n\t"
    "ds_read_b128 a[72:75], v169\n\t"
    "ds_read_b64_tr_b16 v[112:113], v171 offset:4096\n\t"
    "ds_read_b64_tr_b16 v[114:115], v171 offset:4608\n\t"
    "ds_read_b64_tr_b16 v[116:117], v172 offset:4096\n\t"
    "s_add_u32 m0, s53, 58368\n\t"
    "v_mfma_f32_32x32x16_bf16 v[64:79], v[132:135], a[48:51], v[64:79]\n\t"
    "buffer_load_dwordx4 v4, s[24:27], s52 offen lds\n\t"
    "ds_read_b64_tr_b16 v[118:119], v172 offset:4608\n\t"
    "ds_read_b128 a[76:79], v170\n\t"
    "ds_read_b64_tr_b16 v[120:121], v171 offset:6144\n\t"
    "s_add_u32 m0, s53, 66432\n\t"
    "v_mfma_f32_32x32x16_bf16 v[48:63], v[136:139], a[52:55], v[48:63]\n\t"
    "buffer_load_dwordx4 v4, s[24:27], s52 offen offset:128 lds\n\t"
    "ds_read_b64_tr_b16 v[122:123], v171 offset:6656\n\t"
    "ds_read_b64_tr_b16 v[124:125], v172 offset:6144\n\t"
    "ds_read_b64_tr_b16 v[126:127], v172 offset:6656\n\t"
    "v_mfma_f32_32x32x16_bf16 v[64:79], v[140:143], a[52:55], v[64:79]\n\t"
    "s_add_u32 s50, s50, 128\n\t"
    "s_cmp_eq_u32 s50, 1280\n\t"
    "s_cselect_b32 s50, 0, s50\n\t"
    "s_add_u32 s51, s51, 196608\n\t"
    "s_cmp_eq_u32 s51, 1966080\n\t"
    "s_cselect_b32 s51, 0, s51\n\t"
    "s_add_u32 s52, s51, 24576\n\t"
    "v_mfma_f32_32x32x16_bf16 v[48:63], v[144:147], a[56:59], v[48:63]\n\t"
    "v_mfma_f32_32x32x16_bf16 v[64:79], v[148:151], a[56:59], v[64:79]\n\t"
    "s_waitcnt vmcnt(19) lgkmcnt(0)\n\t"
    "s_barrier\n\t"
    "s_add_u32 m0, s53, 73728\n\t"
    "s_nop 0\n\t"
    "buffer_load_dwordx4 v2, s[20:23], s50 offen lds\n\t"
    "ds_read_b128 a[80:83], v167 offset:24576\n\t"
    "ds_read_b64_tr_b16 v[128:129], v171 offset:24576\n\t"
    "ds_read_b64_tr_b16 v[130:131], v171 offset:25088\n\t"
    "ds_read_b64_tr_b16 v[132:133], v172 offset:24576\n\t"
    "s_add_u32 m0, s53, 74752\n\t"
    "v_mfma_f32_32x32x16_bf16 v[48:63], v[152:155], a[60:63], v[48:63]\n\t"
    "buffer_load_dwordx4 v3, s[20:23], s50 offen lds\n\t"
    "ds_read_b64_tr_b16 v[134:135], v172 offset:25088\n\t"
    "ds_read_b128 a[84:87], v168 offset:24576\n\t"
    "ds_read_b64_tr_b16 v[136:137], v171 offset:26624\n\t"
    "s_add_u32 m0, s53, 81920\n\t"
    "v_mfma_f32_32x32x16_bf16 v[64:79], v[156:159], a[60:63], v[64:79]\n\t"
    "buffer_load_dwordx4 v4, s[24:27], s51 offen lds\n\t"
    "ds_read_b64_tr_b16 v[138:139], v171 offset:27136\n\t"
    "ds_read_b64_tr_b16 v[140:141], v172 offset:26624\n\t"
    "ds_read_b64_tr_b16 v[142:143], v172 offset:27136\n\t"
    "s_add_u32 m0, s53, 89984\n\t"
    "v_mfma_f32_32x32x16_bf16 v[48:63], v[96:99], a[64:67], v[48:63]\n\t"
    "buffer_load_dwordx4 v4, s[24:27], s51 offen offset:128 lds\n\t"
    "ds_read_b128 a[88:91], v169 offset:24576\n\t"
    "ds_read_b64_tr_b16 v[144:145], v171 offset:28672\n\t"
    "ds_read_b64_tr_b16 v[146:147], v171 offset:29184\n\t"
    "ds_read_b64_tr_b16 v[148:149], v172 offset:28672\n\t"
    "s_add_u32 m0, s53, 82944\n\t"
    "v_mfma_f32_32x32x16_bf16 v[64:79], v[100:103], a[64:67], v[64:79]\n\t"
    "buffer_load_dwordx4 v4, s[24:27], s52 offen lds\n\t"
    "ds_read_b64_tr_b16 v[150:151], v172 offset:29184\n\t"
    "ds_read_b128 a[92:95], v170 offset:24576\n\t"
    "ds_read_b64_tr_b16 v[152:153], v171 offset:30720\n\t"
    "s_add_u32 m0, s53, 91008\n\t"
    "v_mfma_f32_32x32x16_bf16 v[48:63], v[104:107], a[68:71], v[48:63]\n\t"
    "buffer_load_dwordx4 v4, s[24:27], s52 offen offset:128 lds\n\t"
    "ds_read_b64_tr_b16 v[154:155], v171 offset:31232\n\t"
    "ds_read_b64_tr_b16 v[156:157], v172 offset:30720\n\t"
    "ds_read_b64_tr_b16 v[158:159], v172 offset:31232\n\t"
    "v_mfma_f32_32x32x16_bf16 v[64:79], v[108:111], a[68:71], v[64:79]\n\t"
    "s_mov_b32 s51, s75\n\t"
    "s_add_u32 s52, s51, 24576\n\t"
    "v_mfma_f32_32x32x16_bf16 v[48:63], v[112:115], a[72:75], v[48:63]\n\t"
    "v_mfma_f32_32x32x16_bf16 v[64:79], v[116:119], a[72:75], v[64:79]\n\t"
    "s_waitcnt vmcnt(19) lgkmcnt(0)\n\t"
    "s_barrier\n\t"
    "s_add_u32 m0, s53, 106240\n\t"
    "s_nop 0\n\t"
    "buffer_load_dwordx4 v4, s[24:27], s51 offen offset:256 lds\n\t"
    "ds_read_b128 a[96:99], v5\n\t"
    "ds_read_b64_tr_b16 v[96:97], v9 offset:0\n\t"
    "ds_read_b64_tr_b16 v[98:99], v9 offset:512\n\t"
    "ds_read_b64_tr_b16 v[100:101], v10 offset:0\n\t"
    "s_add_u32 m0, s53, 107264\n\t"
    "v_mfma_f32_32x32x16_bf16 v[48:63], v[120:123], a[76:79], v[48:63]\n\t"
    "buffer_load_dwordx4 v4, s[24:27], s52 offen offset:256 lds\n\t"
    "ds_read_b64_tr_b16 v[102:103], v10 offset:512\n\t"
    "ds_read_b128 a[100:103], v6\n\t"
    "ds_read_b64_tr_b16 v[104:105], v9 offset:2048\n\t"
    "v_mfma_f32_32x32x16_bf16 v[64:79], v[124:127], a[76:79], v[64:79]\n\t"
    "s_add_u32 s51, s51, 196608\n\t"
    "s_cmp_eq_u32 s51, 1966080\n\t"
    "s_cselect_b32 s51, 0, s51\n\t"
    "s_add_u32 s52, s51, 24576\n\t"
    "ds_read_b64_tr_b16 v[106:107], v9 offset:2560\n\t"
    "ds_read_b64_tr_b16 v[108:109], v10 offset:2048\n\t"
    "ds_read_b64_tr_b16 v[110:111], v10 offset:2560\n\t"
    "v_mfma_f32_32x32x16_bf16 v[48:63], v[128:131], a[80:83], v[48:63]\n\t"
    "ds_read_b128 a[104:107], v7\n\t"
    "ds_read_b64_tr_b16 v[112:113], v9 offset:4096\n\t"
    "ds_read_b64_tr_b16 v[114:115], v9 offset:4608\n\t"
    "ds_read_b64_tr_b16 v[116:117], v10 offset:4096\n\t"
    "s_add_u32 m0, s53, 114432\n\t"
    "v_mfma_f32_32x32x16_bf16 v[64:79], v[132:135], a[80:83], v[64:79]\n\t"
    "buffer_load_dwordx4 v4, s[24:27], s51 offen offset:256 lds\n\t"
    "ds_read_b64_tr_b16 v[118:119], v10 offset:4608\n\t"
    "ds_read_b128 a[108:111], v8\n\t"
    "ds_read_b64_tr_b16 v[120:121], v9 offset:6144\n\t"
    "s_add_u32 m0, s53, 115456\n\t"
    "v_mfma_f32_32x32x16_bf16 v[48:63], v[136:139], a[84:87], v[48:63]\n\t"
    "buffer_load_dwordx4 v4, s[24:27], s52 offen offset:256 lds\n\t"
    "ds_read_b64_tr_b16 v[122:123], v9 offset:6656\n\t"
    "ds_read_b64_tr_b16 v[124:125], v10 offset:6144\n\t"
    "ds_read_b64_tr_b16 v[126:127], v10 offset:6656\n\t"
    "v_mfma_f32_32x32x16_bf16 v[64:79], v[140:143], a[84:87], v[64:79]\n\t"
    "s_add_u32 s51, s51, 196608\n\t"
    "s_cmp_eq_u32 s51, 1966080\n\t"
    "s_cselect_b32 s51, 0, s51\n\t"
    "s_add_u32 s52, s51, 24576\n\t"
    "v_mfma_f32_32x32x16_bf16 v[48:63], v[144:147], a[88:91], v[48:63]\n\t"
    "v_mfma_f32_32x32x16_bf16 v[64:79], v[148:151], a[88:91], v[64:79]\n\t"
    "s_waitcnt vmcnt(16) lgkmcnt(0)\n\t"
    "s_barrier\n\t"
    "s_add_u32 m0, s53, 130816\n\t"
    "s_nop 0\n\t"
    "buffer_load_dwordx4 v4, s[24:27], s51 offen offset:256 lds\n\t"
    "ds_read_b128 a[112:115], v5 offset:24576\n\t"
    "ds_read_b64_tr_b16 v[128:129], v9 offset:24576\n\t"
    "ds_read_b64_tr_b16 v[130:131], v9 offset:25088\n\t"
    "ds_read_b64_tr_b16 v[132:133], v10 offset:24576\n\t"
    "s_add_u32 m0, s53, 131840\n\t"
    "v_mfma_f32_32x32x16_bf16 v[48:63], v[152:155], a[92:95], v[48:63]\n\t"
    "buffer_load_dwordx4 v4, s[24:27], s52 offen offset:256 lds\n\t"
    "ds_read_b64_tr_b16 v[134:135], v10 offset:25088\n\t"
    "ds_read_b128 a[116:119], v6 offset:24576\n\t"
    "ds_read_b64_tr_b16 v[136:137], v9 offset:26624\n\t"
    "v_mfma_f32_32x32x16_bf16 v[64:79], v[156:159], a[92:95], v[64:79]\n\t"
    "s_add_u32 s51, s51, 196608\n\t"
    "s_cmp_eq_u32 s51, 1966080\n\t"
    "s_cselect_b32 s51, 0, s51\n\t"
    "s_add_u32 s52, s51, 24576\n\t"
    "ds_read_b64_tr_b16 v[138:139], v9 offset:27136\n\t"
    "ds_read_b64_tr_b16 v[140:141], v10 offset:26624\n\t"
    "ds_read_b64_tr_b16 v[142:143], v10 offset:27136\n\t"
    "v_mfma_f32_32x32x16_bf16 v[48:63], v[96:99], a[96:99], v[48:63]\n\t"
    "ds_read_b128 a[120:123], v7 offset:24576\n\t"
    "ds_read_b64_tr_b16 v[144:145], v9 offset:28672\n\t"
    "ds_read_b64_tr_b16 v[146:147], v9 offset:29184\n\t"
    "ds_read_b64_tr_b16 v[148:149], v10 offset:28672\n\t"
    "s_add_u32 m0, s53, 139008\n\t"
    "v_mfma_f32_32x32x16_bf16 v[64:79], v[100:103], a[96:99], v[64:79]\n\t"
    "buffer_load_dwordx4 v4, s[24:27], s51 offen offset:256 lds\n\t"
    "ds_read_b64_tr_b16 v[150:151], v10 offset:29184\n\t"
    "ds_read_b128 a[124:127], v8 offset:24576\n\t"
    "ds_read_b64_tr_b16 v[152:153], v9 offset:30720\n\t"
    "s_add_u32 m0, s53, 140032\n\t"
    "v_mfma_f32_32x32x16_bf16 v[48:63], v[104:107], a[100:103], v[48:63]\n\t"
    "buffer_load_dwordx4 v4, s[24:27], s52 offen offset:256 lds\n\t"
    "ds_read_b64_tr_b16 v[154:155], v9 offset:31232\n\t"
    "ds_read_b64_tr_b16 v[156:157], v10 offset:30720\n\t"
    "ds_read_b64_tr_b16 v[158:159], v10 offset:31232\n\t"
    "v_mfma_f32_32x32x16_bf16 v[64:79], v[108:111], a[100:103], v[64:79]\n\t"
    "s_add_u32 s51, s51, 196608\n\t"
    "s_cmp_eq_u32 s51, 1966080\n\t"
    "s_cselect_b32 s51, 0, s51\n\t"
    "s_add_u32 s52, s51, 24576\n\t"
    "v_mfma_f32_32x32x16_bf16 v[48:63], v[112:115], a[104:107], v[48:63]\n\t"
    "v_mfma_f32_32x32x16_bf16 v[64:79], v[116:119], a[104:107], v[64:79]\n\t"
    "s_waitcnt vmcnt(14) lgkmcnt(0)\n\t"
    "s_barrier\n\t"
    "buffer_load_dwordx2 v[38:39], v21, s[36:39], s19 offen sc1\n\t"
    "s_add_u32 m0, s53, 7936\n\t"
    "s_nop 0\n\t"
    "buffer_load_dwordx4 v4, s[24:27], s51 offen offset:256 lds\n\t"
    "ds_read_b128 a[128:131], v160\n\t"
    "ds_read_b64_tr_b16 v[96:97], v164 offset:0\n\t"
    "ds_read_b64_tr_b16 v[98:99], v164 offset:512\n\t"
    "ds_read_b64_tr_b16 v[100:101], v165 offset:0\n\t"
    "s_add_u32 m0, s53, 8960\n\t"
    "v_mfma_f32_32x32x16_bf16 v[48:63], v[120:123], a[108:111], v[48:63]\n\t"
    "buffer_load_dwordx4 v4, s[24:27], s52 offen offset:256 lds\n\t"
    "ds_read_b64_tr_b16 v[102:103], v165 offset:512\n\t"
    "ds_read_b128 a[132:135], v161\n\t"
    "ds_read_b64_tr_b16 v[104:105], v164 offset:2048\n\t"
    "v_mfma_f32_32x32x16_bf16 v[64:79], v[124:127], a[108:111], v[64:79]\n\t"
    "s_add_u32 s51, s51, 196608\n\t"
    "s_cmp_eq_u32 s51, 1966080\n\t"
    "s_cselect_b32 s51, 0, s51\n\t"
    "s_add_u32 s52, s51, 24576\n\t"
    "ds_read_b64_tr_b16 v[106:107], v164 offset:2560\n\t"
    "ds_read_b64_tr_b16 v[108:109], v165 offset:2048\n\t"
    "ds_read_b64_tr_b16 v[110:111], v165 offset:2560\n\t"
    "v_mfma_f32_32x32x16_bf16 v[48:63], v[128:131], a[112:115], v[48:63]\n\t"
    "ds_read_b128 a[136:139], v162\n\t"
    "ds_read_b64_tr_b16 v[112:113], v164 offset:4096\n\t"
    "ds_read_b64_tr_b16 v[114:115], v164 offset:4608\n\t"
    "ds_read_b64_tr_b16 v[116:117], v165 offset:4096\n\t"
    "s_add_u32 m0, s53, 16128\n\t"
    "v_mfma_f32_32x32x16_bf16 v[64:79], v[132:135], a[112:115], v[64:79]\n\t"
    "buffer_load_dwordx4 v4, s[24:27], s51 offen offset:256 lds\n\t"
    "ds_read_b64_tr_b16 v[118:119], v165 offset:4608\n\t"
    "ds_read_b128 a[140:143], v163\n\t"
    "ds_read_b64_tr_b16 v[120:121], v164 offset:6144\n\t"
    "s_add_u32 m0, s53, 17152\n\t"
    "v_mfma_f32_32x32x16_bf16 v[48:63], v[136:139], a[116:119], v[48:63]\n\t"
    "buffer_load_dwordx4 v4, s[24:27], s52 offen offset:256 lds\n\t"
    "ds_read_b64_tr_b16 v[122:123], v164 offset:6656\n\t"
    "ds_read_b64_tr_b16 v[124:125], v165 offset:6144\n\t"
    "ds_read_b64_tr_b16 v[126:127], v165 offset:6656\n\t"
    "v_mfma_f32_32x32x16_bf16 v[64:79], v[140:143], a[116:119], v[64:79]\n\t"
    "s_add_u32 s51, s51, 196608\n\t"
    "s_cmp_eq_u32 s51, 1966080\n\t"
    "s_cselect_b32 s51, 0, s51\n\t"
    "s_add_u32 s52, s51, 24576\n\t"
    "v_mfma_f32_32x32x16_bf16 v[48:63], v[144:147], a[120:123], v[48:63]\n\t"
    "v_mfma_f32_32x32x16_bf16 v[64:79], v[148:151], a[120:123], v[64:79]\n\t"
    "s_waitcnt vmcnt(13) lgkmcnt(0)\n\t"
    "s_barrier\n\t"
    "s_add_u32 m0, s53, 32512\n\t"
    "s_nop 0\n\t"
    "buffer_load_dwordx4 v4, s[24:27], s51 offen offset:256 lds\n\t"
    "ds_read_b128 a[144:147], v160 offset:24576\n\t"
    "ds_read_b64_tr_b16 v[128:129], v164 offset:24576\n\t"
    "ds_read_b64_tr_b16 v[130:131], v164 offset:25088\n\t"
    "ds_read_b64_tr_b16 v[132:133], v165 offset:24576\n\t"
    "s_add_u32 m0, s53, 33536\n\t"
    "v_mfma_f32_32x32x16_bf16 v[48:63], v[152:155], a[124:127], v[48:63]\n\t"
    "buffer_load_dwordx4 v4, s[24:27], s52 offen offset:256 lds\n\t"
    "ds_read_b64_tr_b16 v[134:135], v165 offset:25088\n\t"
    "ds_read_b128 a[148:151], v161 offset:24576\n\t"
    "ds_read_b64_tr_b16 v[136:137], v164 offset:26624\n\t"
    "v_mfma_f32_32x32x16_bf16 v[64:79], v[156:159], a[124:127], v[64:79]\n\t"
    "s_add_u32 s51, s51, 196608\n\t"
    "s_cmp_eq_u32 s51, 1966080\n\t"
    "s_cselect_b32 s51, 0, s51\n\t"
    "s_add_u32 s52, s51, 24576\n\t"
    "ds_read_b64_tr_b16 v[138:139], v164 offset:27136\n\t"
    "ds_read_b64_tr_b16 v[140:141], v165 offset:26624\n\t"
    "ds_read_b64_tr_b16 v[142:143], v165 offset:27136\n\t"
    "v_mfma_f32_32x32x16_bf16 v[48:63], v[96:99], a[128:131], v[48:63]\n\t"
    "ds_read_b128 a[152:155], v162 offset:24576\n\t"
    "ds_read_b64_tr_b16 v[144:145], v164 offset:28672\n\t"
    "ds_read_b64_tr_b16 v[146:147], v164 offset:29184\n\t"
    "ds_read_b64_tr_b16 v[148:149], v165 offset:28672\n\t"
    "s_add_u32 m0, s53, 40704\n\t"
    "v_mfma_f32_32x32x16_bf16 v[64:79], v[100:103], a[128:131], v[64:79]\n\t"
    "buffer_load_dwordx4 v4, s[24:27], s51 offen offset:256 lds\n\t"
    "ds_read_b64_tr_b16 v[150:151], v165 offset:29184\n\t"
    "ds_read_b128 a[156:159], v163 offset:24576\n\t"
    "ds_read_b64_tr_b16 v[152:153], v164 offset:30720\n\t"
    "s_add_u32 m0, s53, 41728\n\t"
    "v_mfma_f32_32x32x16_bf16 v[48:63], v[104:107], a[132:135], v[48:63]\n\t"
    "buffer_load_dwordx4 v4, s[24:27], s52 offen offset:256 lds\n\t"
    "ds_read_b64_tr_b16 v[154:155], v164 offset:31232\n\t"
    "ds_read_b64_tr_b16 v[156:157], v165 offset:30720\n\t"
    "ds_read_b64_tr_b16 v[158:159], v165 offset:31232\n\t"
    "v_mfma_f32_32x32x16_bf16 v[64:79], v[108:111], a[132:135], v[64:79]\n\t"
    "s_add_u32 s51, s51, 196608\n\t"
    "s_cmp_eq_u32 s51, 1966080\n\t"
    "s_cselect_b32 s51, 0, s51\n\t"
    "s_add_u32 s52, s51, 24576\n\t"
    "v_mfma_f32_32x32x16_bf16 v[48:63], v[112:115], a[136:139], v[48:63]\n\t"
    "v_mfma_f32_32x32x16_bf16 v[64:79], v[116:119], a[136:139], v[64:79]\n\t"
    "s_waitcnt vmcnt(13) lgkmcnt(0)\n\t"
    "s_barrier\n\t"
    "s_add_u32 m0, s53, 57088\n\t"
    "s_nop 0\n\t"
    "buffer_load_dwordx4 v4, s[24:27], s51 offen offset:256 lds\n\t"
    "ds_read_b64_tr_b16 v[96:97], v173 offset:0\n\t"
    "ds_read_b64_tr_b16 v[98:99], v173 offset:512\n\t"
    "ds_read_b64_tr_b16 v[100:101], v173 offset:2048\n\t"
    "s_add_u32 m0, s53, 58112\n\t"
    "v_mfma_f32_32x32x16_bf16 v[48:63], v[120:123], a[140:143], v[48:63]\n\t"
    "buffer_load_dwordx4 v4, s[24:27], s52 offen offset:256 lds\n\t"
    "ds_read_b64_tr_b16 v[102:103], v173 offset:2560\n\t"
    "ds_read_b64_tr_b16 v[104:105], v173 offset:4096\n\t"
    "ds_read_b64_tr_b16 v[106:107], v173 offset:4608\n\t"
    "v_mfma_f32_32x32x16_bf16 v[64:79], v[124:127], a[140:143], v[64:79]\n\t"
    "s_add_u32 s51, s51, 196608\n\t"
    "s_cmp_eq_u32 s51, 1966080\n\t"
    "s_cselect_b32 s51, 0, s51\n\t"
    "s_add_u32 s52, s51, 24576\n\t"
    "ds_read_b64_tr_b16 v[108:109], v173 offset:6144\n\t"
    "ds_read_b64_tr_b16 v[110:111], v173 offset:6656\n\t"
    "v_mfma_f32_32x32x16_bf16 v[48:63], v[128:131], a[144:147], v[48:63]\n\t"
    "ds_read_b64_tr_b16 v[112:113], v173 offset:8192\n\t"
    "ds_read_b64_tr_b16 v[114:115], v173 offset:8704\n\t"
    "ds_read_b64_tr_b16 v[116:117], v173 offset:10240\n\t"
    "s_add_u32 m0, s53, 65280\n\t"
    "v_mfma_f32_32x32x16_bf16 v[48:63], v[136:139], a[148:151], v[48:63]\n\t"
    "buffer_load_dwordx4 v4, s[24:27], s51 offen offset:256 lds\n\t"
    "ds_read_b64_tr_b16 v[118:119], v173 offset:10752\n\t"
    "ds_read_b64_tr_b16 v[120:121], v173 offset:12288\n\t"
    "ds_read_b64_tr_b16 v[122:123], v173 offset:12800\n\t"
    "s_add_u32 m0, s53, 66304\n\t"
    "v_mfma_f32_32x32x16_bf16 v[48:63], v[144:147], a[152:155], v[48:63]\n\t"
    "buffer_load_dwordx4 v4, s[24:27], s52 offen offset:256 lds\n\t"
    "ds_read_b64_tr_b16 v[124:125], v173 offset:14336\n\t"
    "ds_read_b64_tr_b16 v[126:127], v173 offset:14848\n\t"
    "v_mfma_f32_32x32x16_bf16 v[48:63], v[152:155], a[156:159], v[48:63]\n\t"
    "s_add_u32 s51, s51, 196608\n\t"
    "s_cmp_eq_u32 s51, 1966080\n\t"
    "s_cselect_b32 s51, 0, s51\n\t"
    "s_add_u32 s52, s51, 24576\n\t"
    "v_mfma_f32_32x32x16_bf16 v[64:79], v[132:135], a[144:147], v[64:79]\n\t"
    "v_mfma_f32_32x32x16_bf16 v[64:79], v[140:143], a[148:151], v[64:79]\n\t"
    "s_waitcnt vmcnt(13) lgkmcnt(0)\n\t"
    "s_waitcnt vmcnt(12)\n\t"
    "v_cmp_eq_u64 s[68:69], v[38:39], v[34:35]\n\t"
    "s_cmp_eq_u64 s[68:69], exec\n\t"
    "s_cbranch_scc1 LWOK%=\n\t"
    "s_mov_b32 s62, 0\n\t"
    "LWSPIN%=:\n\t"
    "buffer_load_dwordx2 v[38:39], v21, s[36:39], s19 offen sc1\n\t"
    "s_waitcnt vmcnt(0)\n\t"
    "v_cmp_eq_u64 s[68:69], v[38:39], v[34:35]\n\t"
    "s_cmp_eq_u64 s[68:69], exec\n\t"
    "s_cbranch_scc1 LWOK%=\n\t"
    "s_add_u32 s62, s62, 1\n\t"
    "s_cmp_lt_u32 s62, 0x8000\n\t"
    "s_cbranch_scc1 LWSPIN%=\n\t"
    "s_branch LEND%=\n\t"
    "LWOK%=:\n\t"
    "s_barrier\n\t"
    "s_add_u32 s59, s64, 0\n\t"
    "buffer_load_dwordx4 a[160:163], v40, s[36:39], s59 offen sc1\n\t"
    "ds_read_b64_tr_b16 v[128:129], v173 offset:24576\n\t"
    "ds_read_b64_tr_b16 v[130:131], v173 offset:25088\n\t"
    "ds_read_b64_tr_b16 v[132:133], v173 offset:26624\n\t"
    "v_maximum3_f32 v48, v48, 0, 0\n\t"
    "v_maximum3_f32 v49, v49, 0, 0\n\t"
    "v_maximum3_f32 v50, v50, 0, 0\n\t"
    "v_maximum3_f32 v51, v51, 0, 0\n\t"
    "v_maximum3_f32 v52, v52, 0, 0\n\t"
    "v_maximum3_f32 v53, v53, 0, 0\n\t"
    "v_mfma_f32_32x32x16_bf16 v[64:79], v[148:151], a[152:155], v[64:79]\n\t"
    "ds_read_b64_tr_b16 v[134:135], v173 offset:27136\n\t"
    "ds_read_b64_tr_b16 v[136:137], v173 offset:28672\n\t"
    "ds_read_b64_tr_b16 v[138:139], v173 offset:29184\n\t"
    "v_maximum3_f32 v54, v54, 0, 0\n\t"
    "v_maximum3_f32 v55, v55, 0, 0\n\t"
    "v_cvt_pk_bf16_f32 v224, v48, v49\n\t"
    "v_cvt_pk_bf16_f32 v225, v50, v51\n\t"
    "v_cvt_pk_bf16_f32 v226, v52, v53\n\t"
    "v_cvt_pk_bf16_f32 v227, v54, v55\n\t"
    "buffer_store_dwordx4 v[224:227], v20, s[36:39], s63 offen sc1\n\t"
    "v_mfma_f32_32x32x16_bf16 v[64:79], v[156:159], a[156:159], v[64:79]\n\t"
    "buffer_load_dwordx4 a[164:167], v41, s[36:39], s59 offen sc1\n\t"
    "ds_read_b64_tr_b16 v[140:141], v173 offset:30720\n\t"
    "ds_read_b64_tr_b16 v[142:143], v173 offset:31232\n\t"
    "v_maximum3_f32 v56, v56, 0, 0\n\t"
    "v_maximum3_f32 v57, v57, 0, 0\n\t"
    "v_maximum3_f32 v58, v58, 0, 0\n\t"
    "v_maximum3_f32 v59, v59, 0, 0\n\t"
    "v_maximum3_f32 v60, v60, 0, 0\n\t"
    "v_maximum3_f32 v61, v61, 0, 0\n\t"
    "v_mfma_f32_32x32x16_bf16 v[80:95], v[96:99], a[0:3], 0\n\t"
    "ds_read_b64_tr_b16 v[144:145], v173 offset:32768\n\t"
    "ds_read_b64_tr_b16 v[146:147], v173 offset:33280\n\t"
    "ds_read_b64_tr_b16 v[148:149], v173 offset:34816\n\t"
    "v_maximum3_f32 v62, v62, 0, 0\n\t"
    "v_maximum3_f32 v63, v63, 0, 0\n\t"
    "v_cvt_pk_bf16_f32 v228, v56, v57\n\t"
    "v_cvt_pk_bf16_f32 v229, v58, v59\n\t"
    "v_cvt_pk_bf16_f32 v230, v60, v61\n\t"
    "v_cvt_pk_bf16_f32 v231, v62, v63\n\t"
    "buffer_store_dwordx4 v[228:231], v20, s[36:39], s63 offen offset:512 sc1\n\t"
    "v_mfma_f32_32x32x16_bf16 v[80:95], v[100:103], a[4:7], v[80:95]\n\t"
    "buffer_load_dwordx4 a[168:171], v42, s[36:39], s59 offen sc1\n\t"
    "ds_read_b64_tr_b16 v[150:151], v173 offset:35328\n\t"
    "ds_read_b64_tr_b16 v[152:153], v173 offset:36864\n\t"
    "ds_read_b64_tr_b16 v[154:155], v173 offset:37376\n\t"
    "v_maximum3_f32 v64, v64, 0, 0\n\t"
    "v_maximum3_f32 v65, v65, 0, 0\n\t"
    "v_maximum3_f32 v66, v66, 0, 0\n\t"
    "v_maximum3_f32 v67, v67, 0, 0\n\t"
    "v_maximum3_f32 v68, v68, 0, 0\n\t"
    "v_maximum3_f32 v69, v69, 0, 0\n\t"
    "v_mfma_f32_32x32x16_bf16 v[80:95], v[104:107], a[8:11], v[80:95]\n\t"
    "ds_read_b64_tr_b16 v[156:157], v173 offset:38912\n\t"
    "ds_read_b64_tr_b16 v[158:159], v173 offset:39424\n\t"
    "v_maximum3_f32 v70, v70, 0, 0\n\t"
    "v_maximum3_f32 v71, v71, 0, 0\n\t"
    "v_cvt_pk_bf16_f32 v232, v64, v65\n\t"
    "v_cvt_pk_bf16_f32 v233, v66, v67\n\t"
    "v_cvt_pk_bf16_f32 v234, v68, v69\n\t"
    "v_cvt_pk_bf16_f32 v235, v70, v71\n\t"
    "buffer_store_dwordx4 v[232:235], v20, s[36:39], s63 offen offset:1024 sc1\n\t"
    "v_mfma_f32_32x32x16_bf16 v[80:95], v[108:111], a[12:15], v[80:95]\n\t"
    "buffer_load_dwordx4 a[172:175], v43, s[36:39], s59 offen sc1\n\t"
    "v_maximum3_f32 v72, v72, 0, 0\n\t"
    "v_maximum3_f32 v73, v73, 0, 0\n\t"
    "v_maximum3_f32 v74, v74, 0, 0\n\t"
    "v_maximum3_f32 v75, v75, 0, 0\n\t"
    "v_maximum3_f32 v76, v76, 0, 0\n\t"
    "v_maximum3_f32 v77, v77, 0, 0\n\t"
    "v_mfma_f32_32x32x16_bf16 v[80:95], v[112:115], a[16:19], v[80:95]\n\t"
    "v_maximum3_f32 v78, v78, 0, 0\n\t"
    "v_maximum3_f32 v79, v79, 0, 0\n\t"
    "v_cvt_pk_bf16_f32 v236, v72, v73\n\t"
    "v_cvt_pk_bf16_f32 v237, v74, v75\n\t"
    "v_cvt_pk_bf16_f32 v238, v76, v77\n\t"
    "v_cvt_pk_bf16_f32 v239, v78, v79\n\t"
    "buffer_store_dwordx4 v[236:239], v20, s[36:39], s63 offen offset:1536 sc1\n\t"
    "v_mfma_f32_32x32x16_bf16 v[80:95], v[116:119], a[20:23], v[80:95]\n\t"
    "s_waitcnt vmcnt(16) lgkmcnt(0)\n\t"
    "s_barrier\n\t"
    "buffer_load_dwordx4 a[176:179], v44, s[36:39], s59 offen sc1\n\t"
    "ds_read_b64_tr_b16 v[96:97], v11 offset:0\n\t"
    "ds_read_b64_tr_b16 v[98:99], v11 offset:512\n\t"
    "ds_read_b64_tr_b16 v[100:101], v11 offset:2048\n\t"
    "v_mfma_f32_32x32x16_bf16 v[80:95], v[120:123], a[24:27], v[80:95]\n\t"
    "ds_read_b64_tr_b16 v[102:103], v11 offset:2560\n\t"
    "ds_read_b64_tr_b16 v[104:105], v11 offset:4096\n\t"
    "ds_read_b64_tr_b16 v[106:107], v11 offset:4608\n\t"
    "v_mfma_f32_32x32x16_bf16 v[80:95], v[124:127], a[28:31], v[80:95]\n\t"
    "s_add_u32 s59, s64, 5120\n\t"
    "buffer_load_dwordx4 a[180:183], v40, s[36:39], s59 offen sc1\n\t"
    "ds_read_b64_tr_b16 v[108:109], v11 offset:6144\n\t"
    "ds_read_b64_tr_b16 v[110:111], v11 offset:6656\n\t"
    "v_mfma_f32_32x32x16_bf16 v[80:95], v[128:131], a[32:35], v[80:95]\n\t"
    "ds_read_b64_tr_b16 v[112:113], v11 offset:8192\n\t"
    "ds_read_b64_tr_b16 v[114:115], v11 offset:8704\n\t"
    "ds_read_b64_tr_b16 v[116:117], v11 offset:10240\n\t"
    "v_mfma_f32_32x32x16_bf16 v[80:95], v[132:135], a[36:39], v[80:95]\n\t"
    "buffer_load_dwordx4 a[184:187], v41, s[36:39], s59 offen sc1\n\t"
    "ds_read_b64_tr_b16 v[118:119], v11 offset:10752\n\t"
    "ds_read_b64_tr_b16 v[120:121], v11 offset:12288\n\t"
    "ds_read_b64_tr_b16 v[122:123], v11 offset:12800\n\t"
    "v_mfma_f32_32x32x16_bf16 v[80:95], v[136:139], a[40:43], v[80:95]\n\t"
    "ds_read_b64_tr_b16 v[124:125], v11 offset:14336\n\t"
    "ds_read_b64_tr_b16 v[126:127], v11 offset:14848\n\t"
    "v_mfma_f32_32x32x16_bf16 v[80:95], v[140:143], a[44:47], v[80:95]\n\t"
    "buffer_load_dwordx4 a[188:191], v42, s[36:39], s59 offen sc1\n\t"
    "v_mfma_f32_32x32x16_bf16 v[80:95], v[144:147], a[48:51], v[80:95]\n\t"
    "v_mfma_f32_32x32x16_bf16 v[80:95], v[148:151], a[52:55], v[80:95]\n\t"
    "s_waitcnt vmcnt(16) lgkmcnt(0)\n\t"
    "s_barrier\n\t"
    "buffer_load_dwordx4 a[192:195], v43, s[36:39], s59 offen sc1\n\t"
    "ds_read_b64_tr_b16 v[128:129], v11 offset:24576\n\t"
    "ds_read_b64_tr_b16 v[130:131], v11 offset:25088\n\t"
    "ds_read_b64_tr_b16 v[132:133], v11 offset:26624\n\t"
    "v_mfma_f32_32x32x16_bf16 v[80:95], v[152:155], a[56:59], v[80:95]\n\t"
    "ds_read_b64_tr_b16 v[134:135], v11 offset:27136\n\t"
    "ds_read_b64_tr_b16 v[136:137], v11 offset:28672\n\t"
    "ds_read_b64_tr_b16 v[138:139], v11 offset:29184\n\t"
    "v_mfma_f32_32x32x16_bf16 v[80:95], v[156:159], a[60:63], v[80:95]\n\t"
    "buffer_load_dwordx4 a[196:199], v44, s[36:39], s59 offen sc1\n\t"
    "ds_read_b64_tr_b16 v[140:141], v11 offset:30720\n\t"
    "ds_read_b64_tr_b16 v[142:143], v11 offset:31232\n\t"
    "v_mfma_f32_32x32x16_bf16 v[80:95], v[96:99], a[64:67], v[80:95]\n\t"
    "ds_read_b64_tr_b16 v[144:145], v11 offset:32768\n\t"
    "ds_read_b64_tr_b16 v[146:147], v11 offset:33280\n\t"
    "ds_read_b64_tr_b16 v[148:149], v11 offset:34816\n\t"
    "v_mfma_f32_32x32x16_bf16 v[80:95], v[100:103], a[68:71], v[80:95]\n\t"
    "s_add_u32 s59, s64, 10240\n\t"
    "buffer_load_dwordx4 a[200:203], v40, s[36:39], s59 offen sc1\n\t"
    "ds_read_b64_tr_b16 v[150:151], v11 offset:35328\n\t"
    "ds_read_b64_tr_b16 v[152:153], v11 offset:36864\n\t"
    "ds_read_b64_tr_b16 v[154:155], v11 offset:37376\n\t"
    "v_mfma_f32_32x32x16_bf16 v[80:95], v[104:107], a[72:75], v[80:95]\n\t"
    "ds_read_b64_tr_b16 v[156:157], v11 offset:38912\n\t"
    "ds_read_b64_tr_b16 v[158:159], v11 offset:39424\n\t"
    "v_mfma_f32_32x32x16_bf16 v[80:95], v[108:111], a[76:79], v[80:95]\n\t"
    "buffer_load_dwordx4 a[204:207], v41, s[36:39], s59 offen sc1\n\t"
    "v_mfma_f32_32x32x16_bf16 v[80:95], v[112:115], a[80:83], v[80:95]\n\t"
    "v_mfma_f32_32x32x16_bf16 v[80:95], v[116:119], a[84:87], v[80:95]\n\t"
    "s_waitcnt vmcnt(16) lgkmcnt(0)\n\t"
    "s_barrier\n\t"
    "buffer_load_dwordx4 a[208:211], v42, s[36:39], s59 offen sc1\n\t"
    "ds_read_b64_tr_b16 v[96:97], v166 offset:0\n\t"
    "ds_read_b64_tr_b16 v[98:99], v166 offset:512\n\t"
    "ds_read_b64_tr_b16 v[100:101], v166 offset:2048\n\t"
    "v_mfma_f32_32x32x16_bf16 v[80:95], v[120:123], a[88:91], v[80:95]\n\t"
    "ds_read_b64_tr_b16 v[102:103], v166 offset:2560\n\t"
    "ds_read_b64_tr_b16 v[104:105], v166 offset:4096\n\t"
    "ds_read_b64_tr_b16 v[106:107], v166 offset:4608\n\t"
    "v_mfma_f32_32x32x16_bf16 v[80:95], v[124:127], a[92:95], v[80:95]\n\t"
    "buffer_load_dwordx4 a[212:215], v43, s[36:39], s59 offen sc1\n\t"
    "ds_read_b64_tr_b16 v[108:109], v166 offset:6144\n\t"
    "ds_read_b64_tr_b16 v[110:111], v166 offset:6656\n\t"
    "v_mfma_f32_32x32x16_bf16 v[80:95], v[128:131], a[96:99], v[80:95]\n\t"
    "ds_read_b64_tr_b16 v[112:113], v166 offset:8192\n\t"
    "ds_read_b64_tr_b16 v[114:115], v166 offset:8704\n\t"
    "ds_read_b64_tr_b16 v[116:117], v166 offset:10240\n\t"
    "v_mfma_f32_32x32x16_bf16 v[80:95], v[132:135], a[100:103], v[80:95]\n\t"
    "buffer_load_dwordx4 a[216:219], v44, s[36:39], s59 offen sc1\n\t"
    "ds_read_b64_tr_b16 v[118:119], v166 offset:10752\n\t"
    "ds_read_b64_tr_b16 v[120:121], v166 offset:12288\n\t"
    "ds_read_b64_tr_b16 v[122:123], v166 offset:12800\n\t"
    "v_mfma_f32_32x32x16_bf16 v[80:95], v[136:139], a[104:107], v[80:95]\n\t"
    "ds_read_b64_tr_b16 v[124:125], v166 offset:14336\n\t"
    "ds_read_b64_tr_b16 v[126:127], v166 offset:14848\n\t"
    "v_mfma_f32_32x32x16_bf16 v[80:95], v[140:143], a[108:111], v[80:95]\n\t"
    "s_add_u32 s59, s64, 15360\n\t"
    "buffer_load_dwordx4 a[220:223], v40, s[36:39], s59 offen sc1\n\t"
    "v_mfma_f32_32x32x16_bf16 v[80:95], v[144:147], a[112:115], v[80:95]\n\t"
    "s_waitcnt vmcnt(12)\n\t"
    "s_mov_b64 s[60:61], exec\n\t"
    "s_mov_b64 exec, 1\n\t"
    "buffer_store_dwordx2 v[34:35], v36, s[36:39], s74 offen sc1\n\t"
    "s_mov_b64 exec, s[60:61]\n\t"
    "v_mfma_f32_32x32x16_bf16 v[80:95], v[148:151], a[116:119], v[80:95]\n\t"
    "s_waitcnt lgkmcnt(0)\n\t"
    "s_barrier\n\t"
    "buffer_load_dwordx4 a[224:227], v41, s[36:39], s59 offen sc1\n\t"
    "v_mfma_f32_32x32x16_bf16 v[80:95], v[152:155], a[120:123], v[80:95]\n\t"
    "v_mfma_f32_32x32x16_bf16 v[80:95], v[156:159], a[124:127], v[80:95]\n\t"
    "buffer_load_dwordx4 a[228:231], v42, s[36:39], s59 offen sc1\n\t"
    "v_mfma_f32_32x32x16_bf16 v[80:95], v[96:99], a[128:131], v[80:95]\n\t"
    "v_mfma_f32_32x32x16_bf16 v[80:95], v[100:103], a[132:135], v[80:95]\n\t"
    "buffer_load_dwordx4 a[232:235], v43, s[36:39], s59 offen sc1\n\t"
    "v_mfma_f32_32x32x16_bf16 v[80:95], v[104:107], a[136:139], v[80:95]\n\t"
    "v_mfma_f32_32x32x16_bf16 v[80:95], v[108:111], a[140:143], v[80:95]\n\t"
    "buffer_load_dwordx4 a[236:239], v44, s[36:39], s59 offen sc1\n\t"
    "v_mfma_f32_32x32x16_bf16 v[80:95], v[112:115], a[144:147], v[80:95]\n\t"
    "v_mfma_f32_32x32x16_bf16 v[80:95], v[116:119], a[148:151], v[80:95]\n\t"
    "v_mfma_f32_32x32x16_bf16 v[80:95], v[120:123], a[152:155], v[80:95]\n\t"
    "v_mfma_f32_32x32x16_bf16 v[80:95], v[124:127], a[156:159], v[80:95]\n\t"
    "s_nop 7\n\t"
    "s_nop 3\n\t"
    "v_maximum3_f32 v80, v80, 0, 0\n\t"
    "v_maximum3_f32 v81, v81, 0, 0\n\t"
    "v_maximum3_f32 v82, v82, 0, 0\n\t"
    "v_maximum3_f32 v83, v83, 0, 0\n\t"
    "v_maximum3_f32 v84, v84, 0, 0\n\t"
    "v_maximum3_f32 v85, v85, 0, 0\n\t"
    "v_maximum3_f32 v86, v86, 0, 0\n\t"
    "v_maximum3_f32 v87, v87, 0, 0\n\t"
    "v_cvt_pk_bf16_f32 v240, v80, v81\n\t"
    "v_cvt_pk_bf16_f32 v241, v82, v83\n\t"
    "v_cvt_pk_bf16_f32 v242, v84, v85\n\t"
    "v_cvt_pk_bf16_f32 v243, v86, v87\n\t"
    "buffer_store_dwordx4 v[240:243], v20, s[36:39], s67 offen sc1\n\t"
    "v_maximum3_f32 v88, v88, 0, 0\n\t"
    "v_maximum3_f32 v89, v89, 0, 0\n\t"
    "v_maximum3_f32 v90, v90, 0, 0\n\t"
    "v_maximum3_f32 v91, v91, 0, 0\n\t"
    "v_maximum3_f32 v92, v92, 0, 0\n\t"
    "v_maximum3_f32 v93, v93, 0, 0\n\t"
    "v_maximum3_f32 v94, v94, 0, 0\n\t"
    "v_maximum3_f32 v95, v95, 0, 0\n\t"
    "v_cvt_pk_bf16_f32 v244, v88, v89\n\t"
    "v_cvt_pk_bf16_f32 v245, v90, v91\n\t"
    "v_cvt_pk_bf16_f32 v246, v92, v93\n\t"
    "v_cvt_pk_bf16_f32 v247, v94, v95\n\t"
    "buffer_store_dwordx4 v[244:247], v20, s[36:39], s67 offen offset:512 sc1\n\t"
    "s_add_u32 s59, s72, 30720\n\t"
    "buffer_load_dwordx4 a[240:243], v40, s[36:39], s59 offen sc1\n\t"
    "buffer_load_dwordx4 a[244:247], v41, s[36:39], s59 offen sc1\n\t"
    "s_lshl_b32 s54, s44, 10\n\t"
    "s_add_u32 s56, s44, 1\n\t"
    "s_and_b32 s56, s56, 3\n\t"
    "s_lshl_b32 s57, s56, 12\n\t"
    "s_add_u32 s57, s57, s54\n\t"
    "v_add_u32 v3, s57, v19\n\t"
    "s_lshl_b32 s57, s44, 12\n\t"
    "s_lshl_b32 s56, s56, 10\n\t"
    "s_add_u32 s57, s57, s56\n\t"
    "v_add_u32 v7, s57, v19\n\t"
    "s_add_u32 s56, s44, 2\n\t"
    "s_and_b32 s56, s56, 3\n\t"
    "s_lshl_b32 s57, s56, 12\n\t"
    "s_add_u32 s57, s57, s54\n\t"
    "v_add_u32 v4, s57, v19\n\t"
    "s_lshl_b32 s57, s44, 12\n\t"
    "s_lshl_b32 s56, s56, 10\n\t"
    "s_add_u32 s57, s57, s56\n\t"
    "v_add_u32 v8, s57, v19\n\t"
    "s_add_u32 s56, s44, 3\n\t"
    "s_and_b32 s56, s56, 3\n\t"
    "s_lshl_b32 s57, s56, 12\n\t"
    "s_add_u32 s57, s57, s54\n\t"
    "v_add_u32 v5, s57, v19\n\t"
    "s_lshl_b32 s57, s44, 12\n\t"
    "s_lshl_b32 s56, s56, 10\n\t"
    "s_add_u32 s57, s57, s56\n\t"
    "v_add_u32 v9, s57, v19\n\t"
    "s_add_u32 s57, s54, 16384\n\t"
    "v_add_u32 v6, s57, v19\n\t"
    "s_mul_i32 s57, s44, 20480\n\t"
    "s_add_u32 s57, s57, 16384\n\t"
    "v_add_u32 v10, s57, v19\n\t"
    "v_and_b32 v11, 15, v1\n\t"
    "v_mul_u32_u24 v11, 0x500, v11\n\t"
    "v_lshrrev_b32 v18, 4, v1\n\t"
    "v_lshl_add_u32 v11, v18, 3, v11\n\t"
    "s_mul_i32 s54, s45, 81920\n\t"
    "s_mul_i32 s55, s46, 160\n\t"
    "s_add_u32 s66, s54, s55\n\t"
    "v_mov_b32 v2, v19\n\t"
    "v_mov_b32 v17, v16\n\t"
    "v_mov_b32 v12, v34\n\t"
    "v_mov_b32 v13, v35\n\t"
    "buffer_load_dwordx4 a[248:251], v42, s[36:39], s59 offen sc1\n\t"
    "buffer_load_dwordx4 a[252:255], v43, s[36:39], s59 offen sc1\n\t"
    "buffer_load_dwordx4 a[0:3], v44, s[36:39], s59 offen sc1\n\t"
    "s_add_u32 s59, s72, 35840\n\t"
    "buffer_load_dwordx4 a[4:7], v40, s[36:39], s59 offen sc1\n\t"
    "buffer_load_dwordx4 a[8:11], v41, s[36:39], s59 offen sc1\n\t"
    "buffer_load_dwordx4 a[12:15], v42, s[36:39], s59 offen sc1\n\t"
    "s_waitcnt vmcnt(8)\n\t"
    "s_add_u32 s54, s74, 8192\n\t"
    "s_mov_b64 s[60:61], exec\n\t"
    "s_mov_b64 exec, 1\n\t"
    "buffer_store_dwordx2 v[34:35], v36, s[36:39], s54 offen sc1\n\t"
    "s_mov_b64 exec, s[60:61]\n\t"
    "buffer_load_dwordx4 a[16:19], v43, s[36:39], s59 offen sc1\n\t"
    "buffer_load_dwordx4 a[20:23], v44, s[36:39], s59 offen sc1\n\t"
    "buffer_load_dwordx2 v[32:33], v22, s[36:39], s19 offen sc1\n\t"
    "s_add_u32 s59, s72, 40960\n\t"
    "buffer_load_dwordx4 a[24:27], v40, s[36:39], s59 offen sc1\n\t"
    "buffer_load_dwordx4 a[28:31], v41, s[36:39], s59 offen sc1\n\t"
    "buffer_load_dwordx4 a[32:35], v42, s[36:39], s59 offen sc1\n\t"
    "buffer_load_dwordx4 a[36:39], v43, s[36:39], s59 offen sc1\n\t"
    "buffer_load_dwordx2 v[38:39], v22, s[36:39], s19 offen sc1\n\t"
    "buffer_load_dwordx4 a[40:43], v44, s[36:39], s59 offen sc1\n\t"
    "s_add_u32 s59, s72, 46080\n\t"
    "buffer_load_dwordx4 a[44:47], v40, s[36:39], s59 offen sc1\n\t"
    "buffer_load_dwordx4 a[48:51], v41, s[36:39], s59 offen sc1\n\t"
    "buffer_load_dwordx4 a[52:55], v42, s[36:39], s59 offen sc1\n\t"
    "s_waitcnt vmcnt(9)\n\t"
    "v_cmp_eq_u64 s[68:69], v[32:33], v[34:35]\n\t"
    "s_cmp_eq_u64 s[68:69], exec\n\t"
    "s_cbranch_scc1 LAOK%=\n\t"
    "s_waitcnt vmcnt(4)\n\t"
    "v_cmp_eq_u64 s[68:69], v[38:39], v[34:35]\n\t"
    "s_cmp_eq_u64 s[68:69], exec\n\t"
    "s_cbranch_scc1 LAOK%=\n\t"
    "s_mov_b32 s62, 0\n\t"
    "LASPIN%=:\n\t"
    "buffer_load_dwordx2 v[38:39], v22, s[36:39], s19 offen sc1\n\t"
    "s_waitcnt vmcnt(0)\n\t"
    "v_cmp_eq_u64 s[68:69], v[38:39], v[34:35]\n\t"
    "s_cmp_eq_u64 s[68:69], exec\n\t"
    "s_cbranch_scc1 LAOK%=\n\t"
    "s_add_u32 s62, s62, 1\n\t"
    "s_cmp_lt_u32 s62, 0x8000\n\t"
    "s_cbranch_scc1 LASPIN%=\n\t"
    "s_branch LEND%=\n\t"
    "LAOK%=:\n\t"
    "buffer_load_dwordx4 a[56:59], v43, s[36:39], s59 offen sc1\n\t"
    "buffer_load_dwordx4 a[60:63], v44, s[36:39], s59 offen sc1\n\t"
    "v_mov_b32 v18, v40\n\t"
    "v_mov_b32 v19, v41\n\t"
    "v_mov_b32 v20, v42\n\t"
    "v_mov_b32 v21, v43\n\t"
    "v_mov_b32 v22, v44\n\t"
    "s_add_u32 s58, s65, 0\n\t"
    "buffer_load_dwordx4 v[104:107], v2, s[36:39], s58 offen sc1\n\t"
    "buffer_load_dwordx4 v[108:111], v2, s[36:39], s58 offen offset:1024 sc1\n\t"
    "buffer_load_dwordx4 v[112:115], v2, s[36:39], s58 offen offset:2048 sc1\n\t"
    "buffer_load_dwordx4 v[116:119], v2, s[36:39], s58 offen offset:3072 sc1\n\t"
    "s_add_u32 s58, s73, 4096\n\t"
    "buffer_load_dwordx4 v[120:123], v2, s[36:39], s58 offen offset:2048 sc1\n\t"
    "buffer_load_dwordx4 v[124:127], v2, s[36:39], s58 offen offset:3072 sc1\n\t"
    "s_add_u32 s58, s73, 8192\n\t"
    "buffer_load_dwordx4 v[128:131], v2, s[36:39], s58 offen sc1\n\t"
    "buffer_load_dwordx4 v[132:135], v2, s[36:39], s58 offen offset:1024 sc1\n\t"
    "s_add_u32 s58, s65, 12288\n\t"
    "buffer_load_dwordx4 v[136:139], v2, s[36:39], s58 offen sc1\n\t"
    "buffer_load_dwordx4 v[140:143], v2, s[36:39], s58 offen offset:1024 sc1\n\t"
    "s_waitcnt vmcnt(8)\n\t"
    "v_mfma_f32_16x16x32_bf16 v[24:27], a[160:163], v[104:107], 0\n\t"
    "v_mfma_f32_16x16x32_bf16 v[28:31], a[164:167], v[104:107], 0\n\t"
    "buffer_load_dwordx4 v[144:147], v2, s[36:39], s58 offen offset:2048 sc1\n\t"
    "v_mfma_f32_16x16x32_bf16 v[32:35], a[168:171], v[104:107], 0\n\t"
    "v_mfma_f32_16x16x32_bf16 v[36:39], a[172:175], v[104:107], 0\n\t"
    "s_add_u32 s59, s64, 20480\n\t"
    "buffer_load_dwordx4 a[64:67], v18, s[36:39], s59 offen sc1\n\t"
    "v_mfma_f32_16x16x32_bf16 v[40:43], a[176:179], v[104:107], 0\n\t"
    "v_mfma_f32_16x16x32_bf16 v[24:27], a[180:183], v[108:111], v[24:27]\n\t"
    "v_mfma_f32_16x16x32_bf16 v[28:31], a[184:187], v[108:111], v[28:31]\n\t"
    "buffer_load_dwordx4 v[148:151], v2, s[36:39], s58 offen offset:3072 sc1\n\t"
    "v_mfma_f32_16x16x32_bf16 v[32:35], a[188:191], v[108:111], v[32:35]\n\t"
    "v_mfma_f32_16x16x32_bf16 v[36:39], a[192:195], v[108:111], v[36:39]\n\t"
    "buffer_load_dwordx4 a[68:71], v19, s[36:39], s59 offen sc1\n\t"
    "v_mfma_f32_16x16x32_bf16 v[40:43], a[196:199], v[108:111], v[40:43]\n\t"
    "s_waitcnt vmcnt(10)\n\t"
    "v_mfma_f32_16x16x32_bf16 v[24:27], a[200:203], v[112:115], v[24:27]\n\t"
    "v_mfma_f32_16x16x32_bf16 v[28:31], a[204:207], v[112:115], v[28:31]\n\t"
    "s_add_u32 s58, s73, 16384\n\t"
    "buffer_load_dwordx4 v[152:155], v2, s[36:39], s58 offen offset:2048 sc1\n\t"
    "v_mfma_f32_16x16x32_bf16 v[32:35], a[208:211], v[112:115], v[32:35]\n\t"
    "v_mfma_f32_16x16x32_bf16 v[36:39], a[212:215], v[112:115], v[36:39]\n\t"
    "buffer_load_dwordx4 a[72:75], v20, s[36:39], s59 offen sc1\n\t"
    "v_mfma_f32_16x16x32_bf16 v[40:43], a[216:219], v[112:115], v[40:43]\n\t"
    "v_mfma_f32_16x16x32_bf16 v[24:27], a[220:223], v[116:119], v[24:27]\n\t"
    "v_mfma_f32_16x16x32_bf16 v[28:31], a[224:227], v[116:119], v[28:31]\n\t"
    "buffer_load_dwordx4 v[156:159], v2, s[36:39], s58 offen offset:3072 sc1\n\t"
    "v_mfma_f32_16x16x32_bf16 v[32:35], a[228:231], v[116:119], v[32:35]\n\t"
    "v_mfma_f32_16x16x32_bf16 v[36:39], a[232:235], v[116:119], v[36:39]\n\t"
    "buffer_load_dwordx4 a[76:79], v21, s[36:39], s59 offen sc1\n\t"
    "v_mfma_f32_16x16x32_bf16 v[40:43], a[236:239], v[116:119], v[40:43]\n\t"
    "s_waitcnt vmcnt(12)\n\t"
    "v_mfma_f32_16x16x32_bf16 v[24:27], a[240:243], v[120:123], v[24:27]\n\t"
    "v_mfma_f32_16x16x32_bf16 v[28:31], a[244:247], v[120:123], v[28:31]\n\t"
    "s_add_u32 s58, s73, 20480\n\t"
    "buffer_load_dwordx4 v[160:163], v2, s[36:39], s58 offen sc1\n\t"
    "v_mfma_f32_16x16x32_bf16 v[32:35], a[248:251], v[120:123], v[32:35]\n\t"
    "v_mfma_f32_16x16x32_bf16 v[36:39], a[252:255], v[120:123], v[36:39]\n\t"
    "buffer_load_dwordx4 a[80:83], v22, s[36:39], s59 offen sc1\n\t"
    "v_mfma_f32_16x16x32_bf16 v[40:43], a[0:3], v[120:123], v[40:43]\n\t"
    "v_mfma_f32_16x16x32_bf16 v[24:27], a[4:7], v[124:127], v[24:27]\n\t"
    "v_mfma_f32_16x16x32_bf16 v[28:31], a[8:11], v[124:127], v[28:31]\n\t"
    "buffer_load_dwordx4 v[164:167], v2, s[36:39], s58 offen offset:1024 sc1\n\t"
    "v_mfma_f32_16x16x32_bf16 v[32:35], a[12:15], v[124:127], v[32:35]\n\t"
    "v_mfma_f32_16x16x32_bf16 v[36:39], a[16:19], v[124:127], v[36:39]\n\t"
    "s_add_u32 s59, s64, 25600\n\t"
    "buffer_load_dwordx4 a[84:87], v18, s[36:39], s59 offen sc1\n\t"
    "v_mfma_f32_16x16x32_bf16 v[40:43], a[20:23], v[124:127], v[40:43]\n\t"
    "s_waitcnt vmcnt(14)\n\t"
    "v_mfma_f32_16x16x32_bf16 v[24:27], a[24:27], v[128:131], v[24:27]\n\t"
    "v_mfma_f32_16x16x32_bf16 v[28:31], a[28:31], v[128:131], v[28:31]\n\t"
    "s_add_u32 s58, s65, 24576\n\t"
    "buffer_load_dwordx4 v[168:171], v2, s[36:39], s58 offen sc1\n\t"
    "v_mfma_f32_16x16x32_bf16 v[32:35], a[32:35], v[128:131], v[32:35]\n\t"
    "v_mfma_f32_16x16x32_bf16 v[36:39], a[36:39], v[128:131], v[36:39]\n\t"
    "buffer_load_dwordx4 a[88:91], v19, s[36:39], s59 offen sc1\n\t"
    "v_mfma_f32_16x16x32_bf16 v[40:43], a[40:43], v[128:131], v[40:43]\n\t"
    "v_mfma_f32_16x16x32_bf16 v[24:27], a[44:47], v[132:135], v[24:27]\n\t"
    "v_mfma_f32_16x16x32_bf16 v[28:31], a[48:51], v[132:135], v[28:31]\n\t"
    "buffer_load_dwordx4 v[172:175], v2, s[36:39], s58 offen offset:1024 sc1\n\t"
    "v_mfma_f32_16x16x32_bf16 v[32:35], a[52:55], v[132:135], v[32:35]\n\t"
    "v_mfma_f32_16x16x32_bf16 v[36:39], a[56:59], v[132:135], v[36:39]\n\t"
    "buffer_load_dwordx4 a[92:95], v20, s[36:39], s59 offen sc1\n\t"
    "v_mfma_f32_16x16x32_bf16 v[40:43], a[60:63], v[132:135], v[40:43]\n\t"
    "s_waitcnt vmcnt(16)\n\t"
    "v_mfma_f32_16x16x32_bf16 v[44:47], a[160:163], v[136:139], 0\n\t"
    "v_mfma_f32_16x16x32_bf16 v[48:51], a[164:167], v[136:139], 0\n\t"
    "buffer_load_dwordx4 v[176:179], v2, s[36:39], s58 offen offset:2048 sc1\n\t"
    "v_mfma_f32_16x16x32_bf16 v[52:55], a[168:171], v[136:139], 0\n\t"
    "v_mfma_f32_16x16x32_bf16 v[56:59], a[172:175], v[136:139], 0\n\t"
    "buffer_load_dwordx4 a[96:99], v21, s[36:39], s59 offen sc1\n\t"
    "v_mfma_f32_16x16x32_bf16 v[60:63], a[176:179], v[136:139], 0\n\t"
    "v_mfma_f32_16x16x32_bf16 v[44:47], a[180:183], v[140:143], v[44:47]\n\t"
    "v_mfma_f32_16x16x32_bf16 v[48:51], a[184:187], v[140:143], v[48:51]\n\t"
    "buffer_load_dwordx4 v[180:183], v2, s[36:39], s58 offen offset:3072 sc1\n\t"
    "v_mfma_f32_16x16x32_bf16 v[52:55], a[188:191], v[140:143], v[52:55]\n\t"
    "v_mfma_f32_16x16x32_bf16 v[56:59], a[192:195], v[140:143], v[56:59]\n\t"
    "buffer_load_dwordx4 a[100:103], v22, s[36:39], s59 offen sc1\n\t"
    "v_mfma_f32_16x16x32_bf16 v[60:63], a[196:199], v[140:143], v[60:63]\n\t"
    "s_waitcnt vmcnt(17)\n\t"
    "v_mfma_f32_16x16x32_bf16 v[44:47], a[200:203], v[144:147], v[44:47]\n\t"
    "v_mfma_f32_16x16x32_bf16 v[48:51], a[204:207], v[144:147], v[48:51]\n\t"
    "buffer_load_dwordx2 v[14:15], v17, s[36:39], s19 offen sc1\n\t"
    "s_add_u32 s58, s73, 28672\n\t"
    "buffer_load_dwordx4 v[184:187], v2, s[36:39], s58 offen offset:2048 sc1\n\t"
    "v_mfma_f32_16x16x32_bf16 v[52:55], a[208:211], v[144:147], v[52:55]\n\t"
    "v_mfma_f32_16x16x32_bf16 v[56:59], a[212:215], v[144:147], v[56:59]\n\t"
    "s_add_u32 s59, s72, 51200\n\t"
    "buffer_load_dwordx4 a[104:107], v18, s[36:39], s59 offen sc1\n\t"
    "v_mfma_f32_16x16x32_bf16 v[60:63], a[216:219], v[144:147], v[60:63]\n\t"
    "v_mfma_f32_16x16x32_bf16 v[44:47], a[220:223], v[148:151], v[44:47]\n\t"
    "v_mfma_f32_16x16x32_bf16 v[48:51], a[224:227], v[148:151], v[48:51]\n\t"
    "buffer_load_dwordx4 v[188:191], v2, s[36:39], s58 offen offset:3072 sc1\n\t"
    "v_mfma_f32_16x16x32_bf16 v[52:55], a[228:231], v[148:151], v[52:55]\n\t"
    "v_mfma_f32_16x16x32_bf16 v[56:59], a[232:235], v[148:151], v[56:59]\n\t"
    "buffer_load_dwordx4 a[108:111], v19, s[36:39], s59 offen sc1\n\t"
    "v_mfma_f32_16x16x32_bf16 v[60:63], a[236:239], v[148:151], v[60:63]\n\t"
    "s_waitcnt vmcnt(18)\n\t"
    "v_mfma_f32_16x16x32_bf16 v[44:47], a[240:243], v[152:155], v[44:47]\n\t"
    "v_mfma_f32_16x16x32_bf16 v[48:51], a[244:247], v[152:155], v[48:51]\n\t"
    "s_add_u32 s58, s73, 32768\n\t"
    "buffer_load_dwordx4 v[192:195], v2, s[36:39], s58 offen sc1\n\t"
    "v_mfma_f32_16x16x32_bf16 v[52:55], a[248:251], v[152:155], v[52:55]\n\t"
    "v_mfma_f32_16x16x32_bf16 v[56:59], a[252:255], v[152:155], v[56:59]\n\t"
    "buffer_load_dwordx4 a[112:115], v20, s[36:39], s59 offen sc1\n\t"
    "v_mfma_f32_16x16x32_bf16 v[60:63], a[0:3], v[152:155], v[60:63]\n\t"
    "v_mfma_f32_16x16x32_bf16 v[44:47], a[4:7], v[156:159], v[44:47]\n\t"
    "v_mfma_f32_16x16x32_bf16 v[48:51], a[8:11], v[156:159], v[48:51]\n\t"
    "buffer_load_dwordx4 v[196:199], v2, s[36:39], s58 offen offset:1024 sc1\n\t"
    "v_mfma_f32_16x16x32_bf16 v[52:55], a[12:15], v[156:159], v[52:55]\n\t"
    "v_mfma_f32_16x16x32_bf16 v[56:59], a[16:19], v[156:159], v[56:59]\n\t"
    "buffer_load_dwordx4 a[116:119], v21, s[36:39], s59 offen sc1\n\t"
    "v_mfma_f32_16x16x32_bf16 v[60:63], a[20:23], v[156:159], v[60:63]\n\t"
    "s_waitcnt vmcnt(18)\n\t"
    "v_mfma_f32_16x16x32_bf16 v[44:47], a[24:27], v[160:163], v[44:47]\n\t"
    "v_mfma_f32_16x16x32_bf16 v[48:51], a[28:31], v[160:163], v[48:51]\n\t"
    "s_add_u32 s58, s65, 36864\n\t"
    "buffer_load_dwordx4 v[200:203], v2, s[36:39], s58 offen sc1\n\t"
    "v_mfma_f32_16x16x32_bf16 v[52:55], a[32:35], v[160:163], v[52:55]\n\t"
    "v_mfma_f32_16x16x32_bf16 v[56:59], a[36:39], v[160:163], v[56:59]\n\t"
    "buffer_load_dwordx4 a[120:123], v22, s[36:39], s59 offen sc1\n\t"
    "v_mfma_f32_16x16x32_bf16 v[60:63], a[40:43], v[160:163], v[60:63]\n\t"
    "v_mfma_f32_16x16x32_bf16 v[44:47], a[44:47], v[164:167], v[44:47]\n\t"
    "v_mfma_f32_16x16x32_bf16 v[48:51], a[48:51], v[164:167], v[48:51]\n\t"
    "buffer_load_dwordx4 v[204:207], v2, s[36:39], s58 offen offset:1024 sc1\n\t"
    "v_mfma_f32_16x16x32_bf16 v[52:55], a[52:55], v[164:167], v[52:55]\n\t"
    "v_mfma_f32_16x16x32_bf16 v[56:59], a[56:59], v[164:167], v[56:59]\n\t"
    "s_add_u32 s59, s72, 56320\n\t"
    "buffer_load_dwordx4 a[124:127], v18, s[36:39], s59 offen sc1\n\t"
    "v_mfma_f32_16x16x32_bf16 v[60:63], a[60:63], v[164:167], v[60:63]\n\t"
    "s_waitcnt vmcnt(18)\n\t"
    "v_mfma_f32_16x16x32_bf16 v[64:67], a[160:163], v[168:171], 0\n\t"
    "v_mfma_f32_16x16x32_bf16 v[68:71], a[164:167], v[168:171], 0\n\t"
    "buffer_load_dwordx4 v[208:211], v2, s[36:39], s58 offen offset:2048 sc1\n\t"
    "v_mfma_f32_16x16x32_bf16 v[72:75], a[168:171], v[168:171], 0\n\t"
    "v_mfma_f32_16x16x32_bf16 v[76:79], a[172:175], v[168:171], 0\n\t"
    "buffer_load_dwordx4 a[128:131], v19, s[36:39], s59 offen sc1\n\t"
    "v_mfma_f32_16x16x32_bf16 v[80:83], a[176:179], v[168:171], 0\n\t"
    "v_mfma_f32_16x16x32_bf16 v[64:67], a[180:183], v[172:175], v[64:67]\n\t"
    "v_mfma_f32_16x16x32_bf16 v[68:71], a[184:187], v[172:175], v[68:71]\n\t"
    "buffer_load_dwordx4 v[212:215], v2, s[36:39], s58 offen offset:3072 sc1\n\t"
    "v_mfma_f32_16x16x32_bf16 v[72:75], a[188:191], v[172:175], v[72:75]\n\t"
    "v_mfma_f32_16x16x32_bf16 v[76:79], a[192:195], v[172:175], v[76:79]\n\t"
    "buffer_load_dwordx4 a[132:135], v20, s[36:39], s59 offen sc1\n\t"
    "v_mfma_f32_16x16x32_bf16 v[80:83], a[196:199], v[172:175], v[80:83]\n\t"
    "s_waitcnt vmcnt(18)\n\t"
    "v_mfma_f32_16x16x32_bf16 v[64:67], a[200:203], v[176:179], v[64:67]\n\t"
    "v_mfma_f32_16x16x32_bf16 v[68:71], a[204:207], v[176:179], v[68:71]\n\t"
    "s_add_u32 s58, s73, 40960\n\t"
    "buffer_load_dwordx4 v[216:219], v2, s[36:39], s58 offen offset:2048 sc1\n\t"
    "v_mfma_f32_16x16x32_bf16 v[72:75], a[208:211], v[176:179], v[72:75]\n\t"
    "v_mfma_f32_16x16x32_bf16 v[76:79], a[212:215], v[176:179], v[76:79]\n\t"
    "buffer_load_dwordx4 a[136:139], v21, s[36:39], s59 offen sc1\n\t"
    "v_mfma_f32_16x16x32_bf16 v[80:83], a[216:219], v[176:179], v[80:83]\n\t"
    "v_mfma_f32_16x16x32_bf16 v[64:67], a[220:223], v[180:183], v[64:67]\n\t"
    "v_mfma_f32_16x16x32_bf16 v[68:71], a[224:227], v[180:183], v[68:71]\n\t"
    "buffer_load_dwordx4 v[220:223], v2, s[36:39], s58 offen offset:3072 sc1\n\t"
    "v_mfma_f32_16x16x32_bf16 v[72:75], a[228:231], v[180:183], v[72:75]\n\t"
    "v_mfma_f32_16x16x32_bf16 v[76:79], a[232:235], v[180:183], v[76:79]\n\t"
    "buffer_load_dwordx4 a[140:143], v22, s[36:39], s59 offen sc1\n\t"
    "v_mfma_f32_16x16x32_bf16 v[80:83], a[236:239], v[180:183], v[80:83]\n\t"
    "s_waitcnt vmcnt(17)\n\t"
    "v_mfma_f32_16x16x32_bf16 v[64:67], a[240:243], v[184:187], v[64:67]\n\t"
    "v_mfma_f32_16x16x32_bf16 v[68:71], a[244:247], v[184:187], v[68:71]\n\t"
    "s_add_u32 s58, s73, 45056\n\t"
    "buffer_load_dwordx4 v[224:227], v2, s[36:39], s58 offen sc1\n\t"
    "v_mfma_f32_16x16x32_bf16 v[72:75], a[248:251], v[184:187], v[72:75]\n\t"
    "v_mfma_f32_16x16x32_bf16 v[76:79], a[252:255], v[184:187], v[76:79]\n\t"
    "v_mfma_f32_16x16x32_bf16 v[80:83], a[0:3], v[184:187], v[80:83]\n\t"
    "v_mfma_f32_16x16x32_bf16 v[64:67], a[4:7], v[188:191], v[64:67]\n\t"
    "v_mfma_f32_16x16x32_bf16 v[68:71], a[8:11], v[188:191], v[68:71]\n\t"
    "buffer_load_dwordx4 v[228:231], v2, s[36:39], s58 offen offset:1024 sc1\n\t"
    "v_mfma_f32_16x16x32_bf16 v[72:75], a[12:15], v[188:191], v[72:75]\n\t"
    "v_mfma_f32_16x16x32_bf16 v[76:79], a[16:19], v[188:191], v[76:79]\n\t"
    "v_mfma_f32_16x16x32_bf16 v[80:83], a[20:23], v[188:191], v[80:83]\n\t"
    "s_waitcnt vmcnt(15)\n\t"
    "v_mfma_f32_16x16x32_bf16 v[64:67], a[24:27], v[192:195], v[64:67]\n\t"
    "v_mfma_f32_16x16x32_bf16 v[68:71], a[28:31], v[192:195], v[68:71]\n\t"
    "s_waitcnt vmcnt(22)\n\t"
    "v_cmp_eq_u64 s[68:69], v[14:15], v[12:13]\n\t"
    "s_cmp_eq_u64 s[68:69], exec\n\t"
    "s_cbranch_scc1 LBOK%=\n\t"
    "s_mov_b32 s62, 0\n\t"
    "LBSPIN%=:\n\t"
    "buffer_load_dwordx2 v[14:15], v17, s[36:39], s19 offen sc1\n\t"
    "s_waitcnt vmcnt(0)\n\t"
    "v_cmp_eq_u64 s[68:69], v[14:15], v[12:13]\n\t"
    "s_cmp_eq_u64 s[68:69], exec\n\t"
    "s_cbranch_scc1 LBOK%=\n\t"
    "s_add_u32 s62, s62, 1\n\t"
    "s_cmp_lt_u32 s62, 0x8000\n\t"
    "s_cbranch_scc1 LBSPIN%=\n\t"
    "s_branch LEND%=\n\t"
    "LBOK%=:\n\t"
    "s_add_u32 s58, s65, 4096\n\t"
    "buffer_load_dwordx4 v[232:235], v2, s[36:39], s58 offen sc1\n\t"
    "v_mfma_f32_16x16x32_bf16 v[72:75], a[32:35], v[192:195], v[72:75]\n\t"
    "v_mfma_f32_16x16x32_bf16 v[76:79], a[36:39], v[192:195], v[76:79]\n\t"
    "v_mfma_f32_16x16x32_bf16 v[80:83], a[40:43], v[192:195], v[80:83]\n\t"
    "v_mfma_f32_16x16x32_bf16 v[64:67], a[44:47], v[196:199], v[64:67]\n\t"
    "v_mfma_f32_16x16x32_bf16 v[68:71], a[48:51], v[196:199], v[68:71]\n\t"
    "buffer_load_dwordx4 v[236:239], v2, s[36:39], s58 offen offset:1024 sc1\n\t"
    "v_mfma_f32_16x16x32_bf16 v[72:75], a[52:55], v[196:199], v[72:75]\n\t"
    "v_mfma_f32_16x16x32_bf16 v[76:79], a[56:59], v[196:199], v[76:79]\n\t"
    "v_mfma_f32_16x16x32_bf16 v[80:83], a[60:63], v[196:199], v[80:83]\n\t"
    "s_waitcnt vmcnt(13)\n\t"
    "v_mfma_f32_16x16x32_bf16 v[84:87], a[160:163], v[200:203], 0\n\t"
    "v_mfma_f32_16x16x32_bf16 v[88:91], a[164:167], v[200:203], 0\n\t"
    "s_or_b32 s54, s47, s44\n\t"
    "s_cmp_lg_u32 s54, 0\n\t"
    "s_cbranch_scc1 LEP1%=\n\t"
    "v_lshlrev_b32 v0, 5, v1\n\t"
    "s_add_u32 s55, s19, 0\n\t"
    "buffer_load_dwordx2 v[16:17], v0, s[36:39], s55 offen sc1\n\t"
    "s_add_u32 s55, s19, 2048\n\t"
    "buffer_load_dwordx2 v[18:19], v0, s[36:39], s55 offen sc1\n\t"
    "s_add_u32 s55, s19, 4096\n\t"
    "buffer_load_dwordx2 v[20:21], v0, s[36:39], s55 offen sc1\n\t"
    "s_add_u32 s55, s19, 6144\n\t"
    "buffer_load_dwordx2 v[22:23], v0, s[36:39], s55 offen sc1\n\t"
    "LEP1%=:\n\t"
    "s_add_u32 s58, s73, 8192\n\t"
    "buffer_load_dwordx4 v[240:243], v2, s[36:39], s58 offen offset:2048 sc1\n\t"
    "v_mfma_f32_16x16x32_bf16 v[92:95], a[168:171], v[200:203], 0\n\t"
    "v_mfma_f32_16x16x32_bf16 v[96:99], a[172:175], v[200:203], 0\n\t"
    "v_mfma_f32_16x16x32_bf16 v[100:103], a[176:179], v[200:203], 0\n\t"
    "v_mfma_f32_16x16x32_bf16 v[84:87], a[180:183], v[204:207], v[84:87]\n\t"
    "v_mfma_f32_16x16x32_bf16 v[88:91], a[184:187], v[204:207], v[88:91]\n\t"
    "buffer_load_dwordx4 v[244:247], v2, s[36:39], s58 offen offset:3072 sc1\n\t"
    "v_mfma_f32_16x16x32_bf16 v[92:95], a[188:191], v[204:207], v[92:95]\n\t"
    "v_mfma_f32_16x16x32_bf16 v[96:99], a[192:195], v[204:207], v[96:99]\n\t"
    "v_mfma_f32_16x16x32_bf16 v[100:103], a[196:199], v[204:207], v[100:103]\n\t"
    "s_waitcnt vmcnt(11)\n\t"
    "v_mfma_f32_16x16x32_bf16 v[84:87], a[200:203], v[208:211], v[84:87]\n\t"
    "v_mfma_f32_16x16x32_bf16 v[88:91], a[204:207], v[208:211], v[88:91]\n\t"
    "s_add_u32 s58, s65, 16384\n\t"
    "buffer_load_dwordx4 v[248:251], v2, s[36:39], s58 offen sc1\n\t"
    "v_mfma_f32_16x16x32_bf16 v[92:95], a[208:211], v[208:211], v[92:95]\n\t"
    "v_mfma_f32_16x16x32_bf16 v[96:99], a[212:215], v[208:211], v[96:99]\n\t"
    "v_mfma_f32_16x16x32_bf16 v[100:103], a[216:219], v[208:211], v[100:103]\n\t"
    "v_mfma_f32_16x16x32_bf16 v[84:87], a[220:223], v[212:215], v[84:87]\n\t"
    "v_mfma_f32_16x16x32_bf16 v[88:91], a[224:227], v[212:215], v[88:91]\n\t"
    "buffer_load_dwordx4 v[252:255], v2, s[36:39], s58 offen offset:1024 sc1\n\t"
    "v_mfma_f32_16x16x32_bf16 v[92:95], a[228:231], v[212:215], v[92:95]\n\t"
    "v_mfma_f32_16x16x32_bf16 v[96:99], a[232:235], v[212:215], v[96:99]\n\t"
    "v_mfma_f32_16x16x32_bf16 v[100:103], a[236:239], v[212:215], v[100:103]\n\t"
    "s_waitcnt vmcnt(9)\n\t"
    "v_mfma_f32_16x16x32_bf16 v[84:87], a[240:243], v[216:219], v[84:87]\n\t"
    "v_mfma_f32_16x16x32_bf16 v[88:91], a[244:247], v[216:219], v[88:91]\n\t"
    "s_add_u32 s58, s73, 20480\n\t"
    "buffer_load_dwordx4 v[104:107], v2, s[36:39], s58 offen offset:2048 sc1\n\t"
    "v_mfma_f32_16x16x32_bf16 v[92:95], a[248:251], v[216:219], v[92:95]\n\t"
    "v_mfma_f32_16x16x32_bf16 v[96:99], a[252:255], v[216:219], v[96:99]\n\t"
    "v_mfma_f32_16x16x32_bf16 v[100:103], a[0:3], v[216:219], v[100:103]\n\t"
    "v_mfma_f32_16x16x32_bf16 v[84:87], a[4:7], v[220:223], v[84:87]\n\t"
    "v_mfma_f32_16x16x32_bf16 v[88:91], a[8:11], v[220:223], v[88:91]\n\t"
    "buffer_load_dwordx4 v[108:111], v2, s[36:39], s58 offen offset:3072 sc1\n\t"
    "v_mfma_f32_16x16x32_bf16 v[92:95], a[12:15], v[220:223], v[92:95]\n\t"
    "v_mfma_f32_16x16x32_bf16 v[96:99], a[16:19], v[220:223], v[96:99]\n\t"
    "v_mfma_f32_16x16x32_bf16 v[100:103], a[20:23], v[220:223], v[100:103]\n\t"
    "s_waitcnt vmcnt(8)\n\t"
    "v_mfma_f32_16x16x32_bf16 v[84:87], a[24:27], v[224:227], v[84:87]\n\t"
    "v_mfma_f32_16x16x32_bf16 v[88:91], a[28:31], v[224:227], v[88:91]\n\t"
    "s_add_u32 s58, s65, 28672\n\t"
    "buffer_load_dwordx4 v[112:115], v2, s[36:39], s58 offen sc1\n\t"
    "v_mfma_f32_16x16x32_bf16 v[92:95], a[32:35], v[224:227], v[92:95]\n\t"
    "v_mfma_f32_16x16x32_bf16 v[96:99], a[36:39], v[224:227], v[96:99]\n\t"
    "v_mfma_f32_16x16x32_bf16 v[100:103], a[40:43], v[224:227], v[100:103]\n\t"
    "v_mfma_f32_16x16x32_bf16 v[84:87], a[44:47], v[228:231], v[84:87]\n\t"
    "v_mfma_f32_16x16x32_bf16 v[88:91], a[48:51], v[228:231], v[88:91]\n\t"
    "buffer_load_dwordx4 v[116:119], v2, s[36:39], s58 offen offset:1024 sc1\n\t"
    "v_mfma_f32_16x16x32_bf16 v[92:95], a[52:55], v[228:231], v[92:95]\n\t"
    "v_mfma_f32_16x16x32_bf16 v[96:99], a[56:59], v[228:231], v[96:99]\n\t"
    "v_mfma_f32_16x16x32_bf16 v[100:103], a[60:63], v[228:231], v[100:103]\n\t"
    "s_waitcnt vmcnt(8)\n\t"
    "v_mfma_f32_16x16x32_bf16 v[24:27], a[64:67], v[232:235], v[24:27]\n\t"
    "v_mfma_f32_16x16x32_bf16 v[28:31], a[68:71], v[232:235], v[28:31]\n\t"
    "s_add_u32 s58, s73, 32768\n\t"
    "buffer_load_dwordx4 v[120:123], v2, s[36:39], s58 offen offset:2048 sc1\n\t"
    "v_mfma_f32_16x16x32_bf16 v[32:35], a[72:75], v[232:235], v[32:35]\n\t"
    "v_mfma_f32_16x16x32_bf16 v[36:39], a[76:79], v[232:235], v[36:39]\n\t"
    "v_mfma_f32_16x16x32_bf16 v[40:43], a[80:83], v[232:235], v[40:43]\n\t"
    "v_mfma_f32_16x16x32_bf16 v[24:27], a[84:87], v[236:239], v[24:27]\n\t"
    "v_mfma_f32_16x16x32_bf16 v[28:31], a[88:91], v[236:239], v[28:31]\n\t"
    "buffer_load_dwordx4 v[124:127], v2, s[36:39], s58 offen offset:3072 sc1\n\t"
    "v_mfma_f32_16x16x32_bf16 v[32:35], a[92:95], v[236:239], v[32:35]\n\t"
    "v_mfma_f32_16x16x32_bf16 v[36:39], a[96:99], v[236:239], v[36:39]\n\t"
    "v_mfma_f32_16x16x32_bf16 v[40:43], a[100:103], v[236:239], v[40:43]\n\t"
    "s_waitcnt vmcnt(8)\n\t"
    "v_mfma_f32_16x16x32_bf16 v[24:27], a[104:107], v[240:243], v[24:27]\n\t"
    "v_mfma_f32_16x16x32_bf16 v[28:31], a[108:111], v[240:243], v[28:31]\n\t"
    "s_add_u32 s58, s65, 40960\n\t"
    "buffer_load_dwordx4 v[128:131], v2, s[36:39], s58 offen sc1\n\t"
    "v_mfma_f32_16x16x32_bf16 v[32:35], a[112:115], v[240:243], v[32:35]\n\t"
    "v_mfma_f32_16x16x32_bf16 v[36:39], a[116:119], v[240:243], v[36:39]\n\t"
    "v_mfma_f32_16x16x32_bf16 v[40:43], a[120:123], v[240:243], v[40:43]\n\t"
    "v_mfma_f32_16x16x32_bf16 v[24:27], a[124:127], v[244:247], v[24:27]\n\t"
    "v_mfma_f32_16x16x32_bf16 v[28:31], a[128:131], v[244:247], v[28:31]\n\t"
    "buffer_load_dwordx4 v[132:135], v2, s[36:39], s58 offen offset:1024 sc1\n\t"
    "v_mfma_f32_16x16x32_bf16 v[32:35], a[132:135], v[244:247], v[32:35]\n\t"
    "v_mfma_f32_16x16x32_bf16 v[36:39], a[136:139], v[244:247], v[36:39]\n\t"
    "v_mfma_f32_16x16x32_bf16 v[40:43], a[140:143], v[244:247], v[40:43]\n\t"
    "s_waitcnt vmcnt(8)\n\t"
    "v_mfma_f32_16x16x32_bf16 v[44:47], a[64:67], v[248:251], v[44:47]\n\t"
    "v_mfma_f32_16x16x32_bf16 v[48:51], a[68:71], v[248:251], v[48:51]\n\t"
    "s_mov_b32 s35, 0\n\t"
    "s_or_b32 s54, s47, s44\n\t"
    "s_cmp_lg_u32 s54, 0\n\t"
    "s_cbranch_scc1 LEP2%=\n\t"
    "s_waitcnt vmcnt(12)\n\t"
    "s_mov_b64 s[60:61], exec\n\t"
    "v_cmp_eq_u64 s[68:69], v[16:17], v[12:13]\n\t"
    "s_and_b64 s[60:61], s[60:61], s[68:69]\n\t"
    "v_cmp_eq_u64 s[68:69], v[18:19], v[12:13]\n\t"
    "s_and_b64 s[60:61], s[60:61], s[68:69]\n\t"
    "v_cmp_eq_u64 s[68:69], v[20:21], v[12:13]\n\t"
    "s_and_b64 s[60:61], s[60:61], s[68:69]\n\t"
    "v_cmp_eq_u64 s[68:69], v[22:23], v[12:13]\n\t"
    "s_and_b64 s[60:61], s[60:61], s[68:69]\n\t"
    "s_cmp_eq_u64 s[60:61], exec\n\t"
    "s_cbranch_scc0 LEP2%=\n\t"
    "v_mov_b32 v16, 0\n\t"
    "s_mov_b64 exec, 1\n\t"
    "buffer_store_dwordx2 v[12:13], v16, s[36:39], 0 offen sc1\n\t"
    "s_mov_b64 exec, -1\n\t"
    "s_mov_b32 s35, 1\n\t"
    "LEP2%=:\n\t"
    "s_add_u32 s58, s73, 45056\n\t"
    "buffer_load_dwordx4 v[136:139], v2, s[36:39], s58 offen offset:2048 sc1\n\t"
    "v_mfma_f32_16x16x32_bf16 v[52:55], a[72:75], v[248:251], v[52:55]\n\t"
    "v_mfma_f32_16x16x32_bf16 v[56:59], a[76:79], v[248:251], v[56:59]\n\t"
    "v_mfma_f32_16x16x32_bf16 v[60:63], a[80:83], v[248:251], v[60:63]\n\t"
    "ds_write_b128 v3, v[28:31] offset:0\n\t"
    "ds_write_b128 v4, v[32:35] offset:0\n\t"
    "ds_write_b128 v5, v[36:39] offset:0\n\t"
    "ds_write_b128 v6, v[40:43] offset:0\n\t"
    "v_mfma_f32_16x16x32_bf16 v[44:47], a[84:87], v[252:255], v[44:47]\n\t"
    "v_mfma_f32_16x16x32_bf16 v[48:51], a[88:91], v[252:255], v[48:51]\n\t"
    "buffer_load_dwordx4 v[140:143], v2, s[36:39], s58 offen offset:3072 sc1\n\t"
    "v_mfma_f32_16x16x32_bf16 v[52:55], a[92:95], v[252:255], v[52:55]\n\t"
    "v_mfma_f32_16x16x32_bf16 v[56:59], a[96:99], v[252:255], v[56:59]\n\t"
    "v_mfma_f32_16x16x32_bf16 v[60:63], a[100:103], v[252:255], v[60:63]\n\t"
    "s_waitcnt vmcnt(8)\n\t"
    "v_mfma_f32_16x16x32_bf16 v[44:47], a[104:107], v[104:107], v[44:47]\n\t"
    "v_mfma_f32_16x16x32_bf16 v[48:51], a[108:111], v[104:107], v[48:51]\n\t"
    "v_mfma_f32_16x16x32_bf16 v[52:55], a[112:115], v[104:107], v[52:55]\n\t"
    "v_mfma_f32_16x16x32_bf16 v[56:59], a[116:119], v[104:107], v[56:59]\n\t"
    "v_mfma_f32_16x16x32_bf16 v[60:63], a[120:123], v[104:107], v[60:63]\n\t"
    "v_mfma_f32_16x16x32_bf16 v[44:47], a[124:127], v[108:111], v[44:47]\n\t"
    "v_mfma_f32_16x16x32_bf16 v[48:51], a[128:131], v[108:111], v[48:51]\n\t"
    "v_mfma_f32_16x16x32_bf16 v[52:55], a[132:135], v[108:111], v[52:55]\n\t"
    "v_mfma_f32_16x16x32_bf16 v[56:59], a[136:139], v[108:111], v[56:59]\n\t"
    "v_mfma_f32_16x16x32_bf16 v[60:63], a[140:143], v[108:111], v[60:63]\n\t"
    "s_waitcnt vmcnt(6)\n\t"
    "v_mfma_f32_16x16x32_bf16 v[64:67], a[64:67], v[112:115], v[64:67]\n\t"
    "v_mfma_f32_16x16x32_bf16 v[68:71], a[68:71], v[112:115], v[68:71]\n\t"
    "v_mfma_f32_16x16x32_bf16 v[72:75], a[72:75], v[112:115], v[72:75]\n\t"
    "v_mfma_f32_16x16x32_bf16 v[76:79], a[76:79], v[112:115], v[76:79]\n\t"
    "v_mfma_f32_16x16x32_bf16 v[80:83], a[80:83], v[112:115], v[80:83]\n\t"
    "ds_write_b128 v3, v[48:51] offset:20480\n\t"
    "ds_write_b128 v4, v[52:55] offset:20480\n\t"
    "ds_write_b128 v5, v[56:59] offset:20480\n\t"
    "ds_write_b128 v6, v[60:63] offset:20480\n\t"
    "v_mfma_f32_16x16x32_bf16 v[64:67], a[84:87], v[116:119], v[64:67]\n\t"
    "v_mfma_f32_16x16x32_bf16 v[68:71], a[88:91], v[116:119], v[68:71]\n\t"
    "v_mfma_f32_16x16x32_bf16 v[72:75], a[92:95], v[116:119], v[72:75]\n\t"
    "v_mfma_f32_16x16x32_bf16 v[76:79], a[96:99], v[116:119], v[76:79]\n\t"
    "v_mfma_f32_16x16x32_bf16 v[80:83], a[100:103], v[116:119], v[80:83]\n\t"
    "s_waitcnt vmcnt(4)\n\t"
    "v_mfma_f32_16x16x32_bf16 v[64:67], a[104:107], v[120:123], v[64:67]\n\t"
    "v_mfma_f32_16x16x32_bf16 v[68:71], a[108:111], v[120:123], v[68:71]\n\t"
    "v_mfma_f32_16x16x32_bf16 v[72:75], a[112:115], v[120:123], v[72:75]\n\t"
    "v_mfma_f32_16x16x32_bf16 v[76:79], a[116:119], v[120:123], v[76:79]\n\t"
    "v_mfma_f32_16x16x32_bf16 v[80:83], a[120:123], v[120:123], v[80:83]\n\t"
    "s_waitcnt lgkmcnt(0)\n\t"
    "s_barrier\n\t"
    "ds_read_b128 v[28:31], v7 offset:0\n\t"
    "ds_read_b128 v[32:35], v8 offset:0\n\t"
    "ds_read_b128 v[36:39], v9 offset:0\n\t"
    "ds_read_b128 v[48:51], v7 offset:20480\n\t"
    "ds_read_b128 v[52:55], v8 offset:20480\n\t"
    "ds_read_b128 v[56:59], v9 offset:20480\n\t"
    "v_mfma_f32_16x16x32_bf16 v[64:67], a[124:127], v[124:127], v[64:67]\n\t"
    "v_mfma_f32_16x16x32_bf16 v[68:71], a[128:131], v[124:127], v[68:71]\n\t"
    "v_mfma_f32_16x16x32_bf16 v[72:75], a[132:135], v[124:127], v[72:75]\n\t"
    "v_mfma_f32_16x16x32_bf16 v[76:79], a[136:139], v[124:127], v[76:79]\n\t"
    "v_mfma_f32_16x16x32_bf16 v[80:83], a[140:143], v[124:127], v[80:83]\n\t"
    "s_waitcnt vmcnt(2)\n\t"
    "v_mfma_f32_16x16x32_bf16 v[84:87], a[64:67], v[128:131], v[84:87]\n\t"
    "v_mfma_f32_16x16x32_bf16 v[88:91], a[68:71], v[128:131], v[88:91]\n\t"
    "v_mfma_f32_16x16x32_bf16 v[92:95], a[72:75], v[128:131], v[92:95]\n\t"
    "v_mfma_f32_16x16x32_bf16 v[96:99], a[76:79], v[128:131], v[96:99]\n\t"
    "v_mfma_f32_16x16x32_bf16 v[100:103], a[80:83], v[128:131], v[100:103]\n\t"
    "ds_write_b128 v3, v[68:71] offset:40960\n\t"
    "ds_write_b128 v4, v[72:75] offset:40960\n\t"
    "ds_write_b128 v5, v[76:79] offset:40960\n\t"
    "ds_write_b128 v6, v[80:83] offset:40960\n\t"
    "v_mfma_f32_16x16x32_bf16 v[84:87], a[84:87], v[132:135], v[84:87]\n\t"
    "v_mfma_f32_16x16x32_bf16 v[88:91], a[88:91], v[132:135], v[88:91]\n\t"
    "v_mfma_f32_16x16x32_bf16 v[92:95], a[92:95], v[132:135], v[92:95]\n\t"
    "v_mfma_f32_16x16x32_bf16 v[96:99], a[96:99], v[132:135], v[96:99]\n\t"
    "v_mfma_f32_16x16x32_bf16 v[100:103], a[100:103], v[132:135], v[100:103]\n\t"
    "s_waitcnt lgkmcnt(0)\n\t"
    "v_add_f32 v24, v24, v28\n\t"
    "v_add_f32 v25, v25, v29\n\t"
    "v_add_f32 v26, v26, v30\n\t"
    "v_add_f32 v27, v27, v31\n\t"
    "v_add_f32 v32, v32, v36\n\t"
    "v_add_f32 v33, v33, v37\n\t"
    "v_add_f32 v34, v34, v38\n\t"
    "v_add_f32 v35, v35, v39\n\t"
    "v_add_f32 v24, v24, v32\n\t"
    "v_add_f32 v25, v25, v33\n\t"
    "v_add_f32 v26, v26, v34\n\t"
    "v_add_f32 v27, v27, v35\n\t"
    "v_cvt_pk_bf16_f32 v28, v24, v25\n\t"
    "v_cvt_pk_bf16_f32 v29, v26, v27\n\t"
    "s_lshl_b32 s54, s44, 5\n\t"
    "s_add_u32 s54, s54, s66\n\t"
    "s_add_u32 s54, s54, 0\n\t"
    "buffer_store_dwordx2 v[28:29], v11, s[40:43], s54 offen\n\t"
    "s_waitcnt vmcnt(1)\n\t"
    "v_mfma_f32_16x16x32_bf16 v[84:87], a[104:107], v[136:139], v[84:87]\n\t"
    "v_mfma_f32_16x16x32_bf16 v[88:91], a[108:111], v[136:139], v[88:91]\n\t"
    "v_mfma_f32_16x16x32_bf16 v[92:95], a[112:115], v[136:139], v[92:95]\n\t"
    "v_mfma_f32_16x16x32_bf16 v[96:99], a[116:119], v[136:139], v[96:99]\n\t"
    "v_mfma_f32_16x16x32_bf16 v[100:103], a[120:123], v[136:139], v[100:103]\n\t"
    "v_add_f32 v44, v44, v48\n\t"
    "v_add_f32 v45, v45, v49\n\t"
    "v_add_f32 v46, v46, v50\n\t"
    "v_add_f32 v47, v47, v51\n\t"
    "v_add_f32 v52, v52, v56\n\t"
    "v_add_f32 v53, v53, v57\n\t"
    "v_add_f32 v54, v54, v58\n\t"
    "v_add_f32 v55, v55, v59\n\t"
    "v_add_f32 v44, v44, v52\n\t"
    "v_add_f32 v45, v45, v53\n\t"
    "v_add_f32 v46, v46, v54\n\t"
    "v_add_f32 v47, v47, v55\n\t"
    "v_cvt_pk_bf16_f32 v48, v44, v45\n\t"
    "v_cvt_pk_bf16_f32 v49, v46, v47\n\t"
    "s_lshl_b32 s54, s44, 5\n\t"
    "s_add_u32 s54, s54, s66\n\t"
    "s_add_u32 s54, s54, 20480\n\t"
    "buffer_store_dwordx2 v[48:49], v11, s[40:43], s54 offen\n\t"
    "s_waitcnt lgkmcnt(0)\n\t"
    "s_barrier\n\t"
    "ds_read_b128 v[68:71], v7 offset:40960\n\t"
    "ds_read_b128 v[72:75], v8 offset:40960\n\t"
    "ds_read_b128 v[76:79], v9 offset:40960\n\t"
    "v_mfma_f32_16x16x32_bf16 v[84:87], a[124:127], v[140:143], v[84:87]\n\t"
    "v_mfma_f32_16x16x32_bf16 v[88:91], a[128:131], v[140:143], v[88:91]\n\t"
    "v_mfma_f32_16x16x32_bf16 v[92:95], a[132:135], v[140:143], v[92:95]\n\t"
    "v_mfma_f32_16x16x32_bf16 v[96:99], a[136:139], v[140:143], v[96:99]\n\t"
    "s_waitcnt lgkmcnt(0)\n\t"
    "v_add_f32 v64, v64, v68\n\t"
    "v_add_f32 v65, v65, v69\n\t"
    "v_add_f32 v66, v66, v70\n\t"
    "v_add_f32 v67, v67, v71\n\t"
    "v_add_f32 v72, v72, v76\n\t"
    "v_add_f32 v73, v73, v77\n\t"
    "v_add_f32 v74, v74, v78\n\t"
    "v_add_f32 v75, v75, v79\n\t"
    "v_add_f32 v64, v64, v72\n\t"
    "v_add_f32 v65, v65, v73\n\t"
    "v_add_f32 v66, v66, v74\n\t"
    "v_add_f32 v67, v67, v75\n\t"
    "v_cvt_pk_bf16_f32 v68, v64, v65\n\t"
    "v_cvt_pk_bf16_f32 v69, v66, v67\n\t"
    "s_lshl_b32 s54, s44, 5\n\t"
    "s_add_u32 s54, s54, s66\n\t"
    "s_add_u32 s54, s54, 40960\n\t"
    "buffer_store_dwordx2 v[68:69], v11, s[40:43], s54 offen\n\t"
    "v_mfma_f32_16x16x32_bf16 v[100:103], a[140:143], v[140:143], v[100:103]\n\t"
    "s_nop 7\n\t"
    "ds_write_b128 v3, v[88:91] offset:61440\n\t"
    "ds_write_b128 v4, v[92:95] offset:61440\n\t"
    "ds_write_b128 v5, v[96:99] offset:61440\n\t"
    "ds_write_b128 v6, v[100:103] offset:61440\n\t"
    "s_waitcnt lgkmcnt(0)\n\t"
    "s_barrier\n\t"
    "ds_read_b128 v[88:91], v7 offset:61440\n\t"
    "ds_read_b128 v[92:95], v8 offset:61440\n\t"
    "ds_read_b128 v[96:99], v9 offset:61440\n\t"
    "ds_read_b128 v[104:107], v10 offset:0\n\t"
    "ds_read_b128 v[108:111], v10 offset:1024\n\t"
    "ds_read_b128 v[112:115], v10 offset:2048\n\t"
    "ds_read_b128 v[116:119], v10 offset:3072\n\t"
    "s_waitcnt lgkmcnt(0)\n\t"
    "v_add_f32 v84, v84, v88\n\t"
    "v_add_f32 v85, v85, v89\n\t"
    "v_add_f32 v86, v86, v90\n\t"
    "v_add_f32 v87, v87, v91\n\t"
    "v_add_f32 v92, v92, v96\n\t"
    "v_add_f32 v93, v93, v97\n\t"
    "v_add_f32 v94, v94, v98\n\t"
    "v_add_f32 v95, v95, v99\n\t"
    "v_add_f32 v84, v84, v92\n\t"
    "v_add_f32 v85, v85, v93\n\t"
    "v_add_f32 v86, v86, v94\n\t"
    "v_add_f32 v87, v87, v95\n\t"
    "v_cvt_pk_bf16_f32 v88, v84, v85\n\t"
    "v_cvt_pk_bf16_f32 v89, v86, v87\n\t"
    "s_lshl_b32 s54, s44, 5\n\t"
    "s_add_u32 s54, s54, s66\n\t"
    "s_add_u32 s54, s54, 61440\n\t"
    "buffer_store_dwordx2 v[88:89], v11, s[40:43], s54 offen\n\t"
    "v_add_f32 v104, v104, v108\n\t"
    "v_add_f32 v105, v105, v109\n\t"
    "v_add_f32 v106, v106, v110\n\t"
    "v_add_f32 v107, v107, v111\n\t"
    "v_add_f32 v112, v112, v116\n\t"
    "v_add_f32 v113, v113, v117\n\t"
    "v_add_f32 v114, v114, v118\n\t"
    "v_add_f32 v115, v115, v119\n\t"
    "v_add_f32 v104, v104, v112\n\t"
    "v_add_f32 v105, v105, v113\n\t"
    "v_add_f32 v106, v106, v114\n\t"
    "v_add_f32 v107, v107, v115\n\t"
    "v_cvt_pk_bf16_f32 v108, v104, v105\n\t"
    "v_cvt_pk_bf16_f32 v109, v106, v107\n\t"
    "s_mul_i32 s54, s44, 20480\n\t"
    "s_add_u32 s54, s54, s66\n\t"
    "buffer_store_dwordx2 v[108:109], v11, s[40:43], s54 offen offset:128\n\t"
    "s_or_b32 s54, s47, s44\n\t"
    "s_or_b32 s54, s54, s35\n\t"
    "s_cmp_lg_u32 s54, 0\n\t"
    "s_cbranch_scc1 LEND%=\n\t"
    "v_lshlrev_b32 v2, 5, v1\n\t"
    "v_add_u32 v2, 8192, v2\n\t"
    "v_mov_b32 v3, 0\n\t"
    "s_mov_b32 s62, 0\n\t"
    "LESPIN%=:\n\t"
    "s_add_u32 s55, s19, 0\n\t"
    "buffer_load_dwordx2 v[20:21], v2, s[36:39], s55 offen sc1\n\t"
    "s_add_u32 s55, s19, 2048\n\t"
    "buffer_load_dwordx2 v[22:23], v2, s[36:39], s55 offen sc1\n\t"
    "s_add_u32 s55, s19, 4096\n\t"
    "buffer_load_dwordx2 v[24:25], v2, s[36:39], s55 offen sc1\n\t"
    "s_add_u32 s55, s19, 6144\n\t"
    "buffer_load_dwordx2 v[26:27], v2, s[36:39], s55 offen sc1\n\t"
    "s_waitcnt vmcnt(0)\n\t"
    "s_mov_b64 s[60:61], exec\n\t"
    "v_cmp_eq_u64 s[68:69], v[20:21], v[12:13]\n\t"
    "s_and_b64 s[60:61], s[60:61], s[68:69]\n\t"
    "v_cmp_eq_u64 s[68:69], v[22:23], v[12:13]\n\t"
    "s_and_b64 s[60:61], s[60:61], s[68:69]\n\t"
    "v_cmp_eq_u64 s[68:69], v[24:25], v[12:13]\n\t"
    "s_and_b64 s[60:61], s[60:61], s[68:69]\n\t"
    "v_cmp_eq_u64 s[68:69], v[26:27], v[12:13]\n\t"
    "s_and_b64 s[60:61], s[60:61], s[68:69]\n\t"
    "s_cmp_eq_u64 s[60:61], exec\n\t"
    "s_cbranch_scc1 LEOK%=\n\t"
    "s_add_u32 s62, s62, 1\n\t"
    "s_cmp_lt_u32 s62, 0x8000\n\t"
    "s_cbranch_scc1 LESPIN%=\n\t"
    "s_branch LEND%=\n\t"
    "LEOK%=:\n\t"
    "s_mov_b64 exec, 1\n\t"
    "buffer_store_dwordx2 v[12:13], v3, s[36:39], 0 offen sc1\n\t"
    "s_mov_b64 exec, -1\n\t"
    "LEND%=:"
    :
    : [ka] "s"(ka), [bid] "s"(bid), [tid] "v"(tid)
    : "v1", "v2", "v3", "v4", "v5", "v6", "v7", "v8", "v9", "v10", "v11", "v12", "v13", "v14", "v15", "v16", "v17", "v18", "v19", "v20", "v21", "v22", "v23", "v24", "v25", "v26", "v27", "v28", "v29", "v30", "v31", "v32", "v33", "v34", "v35", "v36", "v37", "v38", "v39", "v40", "v41", "v42", "v43", "v44", "v45", "v46", "v47", "v48", "v49", "v50", "v51", "v52", "v53", "v54", "v55", "v56", "v57", "v58", "v59", "v60", "v61", "v62", "v63", "v64", "v65", "v66", "v67", "v68", "v69", "v70", "v71", "v72", "v73", "v74", "v75", "v76", "v77", "v78", "v79", "v80", "v81", "v82", "v83", "v84", "v85", "v86", "v87", "v88", "v89", "v90", "v91", "v92", "v93", "v94", "v95", "v96", "v97", "v98", "v99", "v100", "v101", "v102", "v103", "v104", "v105", "v106", "v107", "v108", "v109", "v110", "v111", "v112", "v113", "v114", "v115", "v116", "v117", "v118", "v119", "v120", "v121", "v122", "v123", "v124", "v125", "v126", "v127", "v128", "v129", "v130", "v131", "v132", "v133", "v134", "v135", "v136", "v137", "v138", "v139", "v140", "v141", "v142", "v143", "v144", "v145", "v146", "v147", "v148", "v149", "v150", "v151", "v152", "v153", "v154", "v155", "v156", "v157", "v158", "v159", "v160", "v161", "v162", "v163", "v164", "v165", "v166", "v167", "v168", "v169", "v170", "v171", "v172", "v173", "v174", "v175", "v176", "v177", "v178", "v179", "v180", "v181", "v182", "v183", "v184", "v185", "v186", "v187", "v188", "v189", "v190", "v191", "v192", "v193", "v194", "v195", "v196", "v197", "v198", "v199", "v200", "v201", "v202", "v203", "v204", "v205", "v206", "v207", "v208", "v209", "v210", "v211", "v212", "v213", "v214", "v215", "v216", "v217", "v218", "v219", "v220", "v221", "v222", "v223", "v224", "v225", "v226", "v227", "v228", "v229", "v230", "v231", "v232", "v233", "v234", "v235", "v236", "v237", "v238", "v239", "v240", "v241", "v242", "v243", "v244", "v245", "v246", "v247", "v248", "v249", "v250", "v251", "v252", "v253", "v254", "v255", "a0", "a1", "a2", "a3", "a4", "a5", "a6", "a7", "a8", "a9", "a10", "a11", "a12", "a13", "a14", "a15", "a16", "a17", "a18", "a19", "a20", "a21", "a22", "a23", "a24", "a25", "a26", "a27", "a28", "a29", "a30", "a31", "a32", "a33", "a34", "a35", "a36", "a37", "a38", "a39", "a40", "a41", "a42", "a43", "a44", "a45", "a46", "a47", "a48", "a49", "a50", "a51", "a52", "a53", "a54", "a55", "a56", "a57", "a58", "a59", "a60", "a61", "a62", "a63", "a64", "a65", "a66", "a67", "a68", "a69", "a70", "a71", "a72", "a73", "a74", "a75", "a76", "a77", "a78", "a79", "a80", "a81", "a82", "a83", "a84", "a85", "a86", "a87", "a88", "a89", "a90", "a91", "a92", "a93", "a94", "a95", "a96", "a97", "a98", "a99", "a100", "a101", "a102", "a103", "a104", "a105", "a106", "a107", "a108", "a109", "a110", "a111", "a112", "a113", "a114", "a115", "a116", "a117", "a118", "a119", "a120", "a121", "a122", "a123", "a124", "a125", "a126", "a127", "a128", "a129", "a130", "a131", "a132", "a133", "a134", "a135", "a136", "a137", "a138", "a139", "a140", "a141", "a142", "a143", "a144", "a145", "a146", "a147", "a148", "a149", "a150", "a151", "a152", "a153", "a154", "a155", "a156", "a157", "a158", "a159", "a160", "a161", "a162", "a163", "a164", "a165", "a166", "a167", "a168", "a169", "a170", "a171", "a172", "a173", "a174", "a175", "a176", "a177", "a178", "a179", "a180", "a181", "a182", "a183", "a184", "a185", "a186", "a187", "a188", "a189", "a190", "a191", "a192", "a193", "a194", "a195", "a196", "a197", "a198", "a199", "a200", "a201", "a202", "a203", "a204", "a205", "a206", "a207", "a208", "a209", "a210", "a211", "a212", "a213", "a214", "a215", "a216", "a217", "a218", "a219", "a220", "a221", "a222", "a223", "a224", "a225", "a226", "a227", "a228", "a229", "a230", "a231", "a232", "a233", "a234", "a235", "a236", "a237", "a238", "a239", "a240", "a241", "a242", "a243", "a244", "a245", "a246", "a247", "a248", "a249", "a250", "a251", "a252", "a253", "a254", "a255", "s8", "s9", "s10", "s11", "s12", "s13", "s14", "s15", "s16", "s17", "s18", "s19", "s20", "s21", "s22", "s23", "s24", "s25", "s26", "s27", "s28", "s29", "s30", "s31", "s34", "s35", "s36", "s37", "s38", "s39", "s40", "s41", "s42", "s43", "s44", "s45", "s46", "s47", "s48", "s49", "s50", "s51", "s52", "s53", "s54", "s55", "s56", "s57", "s58", "s59", "s60", "s61", "s62", "s63", "s64", "s65", "s66", "s67", "s68", "s69", "s70", "s71", "s72", "s73", "s74", "s75", "s76", "s77", "s78", "s79", "s80", "s81", "s82", "s83", "s84", "s85", "s86", "s87", "s88", "s89", "s90", "s91", "s92", "s93", "s94", "s95", "s96", "s97", "s98", "s99", "vcc", "scc", "memory");
}

extern "C" void kernel_launch(void* const* d_in, const int* in_sizes, int n_in, void* d_out, int out_size, void* d_ws, size_t ws_size,
                              hipStream_t stream) {
  (void)in_sizes; (void)n_in; (void)out_size; (void)ws_size;
  (void)hipFuncSetAttribute((const void*)mlp_fused, hipFuncAttributeMaxDynamicSharedMemorySize, LDS_BYTES);
  mlp_fused<<<256, 256, LDS_BYTES, stream>>>(d_in[0], d_in[1], d_in[2], d_out, d_ws);
}
```
